```python
import torch

def _softmax(x, axis=-1):
    return torch.softmax(x, dim=axis)

def _gelu(x):
    return torch.nn.functional.gelu(x, approximate='tanh')

def _silu(x):
    return torch.nn.functional.silu(x)

def _sigmoid(x):
    return torch.sigmoid(x)

def _layernorm(x, axis=-1, eps=1e-05):
    m = x.mean(dim=axis, keepdim=True)
    v = x.var(dim=axis, keepdim=True, unbiased=False)
    return (x - m) / torch.sqrt(v + eps)

def _scatter_add(tgt, idx, src, axis=0):
    return tgt.clone().index_add_(axis, idx, src)

def _segment_sum(src, seg, n):
    out = torch.zeros((n,) + src.shape[1:], dtype=src.dtype)
    return out.index_add_(0, seg, src)

def _pad(x, pw):
    flat = tuple((v for pair in reversed(pw) for v in pair))
    return torch.nn.functional.pad(x, flat)
kernel_name = 'adaLN_cond__ssm_conv_h3__dense_activation_squared_relu__prompt'

def setup_inputs():

    _w = lambda a: (lambda t: t.to(torch.bfloat16) if t.is_floating_point() else t)(torch.as_tensor(a))

    def _A_setup():
        B, S, D = (1, 2048, 512)
        ks = 4
        return {'q': _w((lambda *s: torch.randn(*s, dtype=torch.bfloat16) * 0.05)(B, S, D)), 'k': _w((lambda *s: torch.randn(*s, dtype=torch.bfloat16) * 0.8)(B, S, D)), 'v': _w((lambda *s: torch.randn(*s, dtype=torch.bfloat16) * 0.8)(B, S, D)), 'shift_k': _w((lambda *s: torch.randn(*s, dtype=torch.bfloat16) * 0.8)(D, ks) * 0.5), 'ssm_k': _w((lambda *s: torch.randn(*s, dtype=torch.bfloat16) * 0.8)(D, S) * (1.0 / S ** 0.5)), 'ssm_D': _w((lambda *s: torch.randn(*s, dtype=torch.bfloat16) * 0.8)(D))}
    _A_in = _A_setup()

    def _B_setup():
        B, S, D, F = (1, 2048, 512, 1376)
        return {'x': _w((lambda *s: torch.randn(*s, dtype=torch.bfloat16) * 0.05)(B, S, D)), 'w1': _w((lambda *s: torch.randn(*s, dtype=torch.bfloat16) * 0.05)(D, F)), 'b1': _w((lambda *s: torch.randn(*s, dtype=torch.bfloat16) * 0.4)(F)), 'w2': _w((lambda *s: torch.randn(*s, dtype=torch.bfloat16) * 0.05)(F, D)), 'b2': _w((lambda *s: torch.randn(*s, dtype=torch.bfloat16) * 3.2)(D))}
    _B_in = _B_setup()
    return {'A_q': _A_in['q'], 'A_k': _A_in['k'], 'A_v': _A_in['v'], 'A_shift_k': _A_in['shift_k'], 'A_ssm_k': _A_in['ssm_k'], 'A_ssm_D': _A_in['ssm_D'], 'B_x': _B_in['x'], 'B_w1': _B_in['w1'], 'B_b1': _B_in['b1'], 'B_w2': _B_in['w2'], 'B_b2': _B_in['b2'], 'c': _w((lambda *s: torch.randn(*s, dtype=torch.bfloat16) * 0.1)(1, 512) * 8.0), 'w_mod': _w((lambda *s: torch.randn(*s, dtype=torch.bfloat16) * 0.1)(512, 6 * 512) * 512 ** (-0.5) * 8.0)}

def reference(A_q, A_k, A_v, A_shift_k, A_ssm_k, A_ssm_D, B_x, B_w1, B_b1, B_w2, B_b2, c, w_mod):

    def _A_ref(q, k, v, shift_k, ssm_k, ssm_D):
        B, S, D = q.shape
        ks = shift_k.shape[-1]
        k_pad = _pad(k, ((0, 0), (ks - 1, 0), (0, 0)))
        k_shift = torch.zeros_like(k)
        for t in range(ks):
            k_shift = k_shift + k_pad[:, t:t + S, :] * shift_k[:, ks - 1 - t][None, None, :]
        kv = k_shift * v
        L = 2 * S
        kv_pad = _pad(kv, ((0, 0), (0, S), (0, 0)))
        kf_pad = _pad(ssm_k, ((0, 0), (0, S)))
        kv_f = (lambda x, axes=(-2, -1): torch.fft.rfft2(x.float(), dim=axes))(kv_pad[:, None, :, :], axes=(1, 2))
        kf_f = (lambda x, axes=(-2, -1): torch.fft.rfft2(x.float(), dim=axes))(kf_pad[:, None, :], axes=(1, 2))
        kf_f = (lambda x, *d: x.permute(*d))(kf_f, 1, 2, 0)
        y_f = kv_f * kf_f[None, :, :, :]
        y = (lambda x, s, axes=(-2, -1): torch.fft.irfft2(x, s=s, dim=axes).to(torch.bfloat16))(y_f, (1, L), axes=(1, 2))[:, 0, :S, :]
        y = y + kv * ssm_D[None, None, :]
        return q * y

    def _B_ref(x, w1, b1, w2, b2):
        h = torch.einsum('bsd,df->bsf', x, w1) + b1
        zero = torch.zeros_like(h)
        r = torch.where(h > zero, h, zero)
        h = r * r
        out = torch.einsum('bsf,fd->bsd', h, w2) + b2
        return out
    x = A_q
    mod = (_silu(c) @ w_mod).reshape(c.shape[0], 6, 1, -1)
    sa, ca, ga, sb, cb, gb = (mod[:, 0].clone(), mod[:, 1].clone(), mod[:, 2].clone(), mod[:, 3].clone(), mod[:, 4].clone(), mod[:, 5].clone())
    h = _layernorm(x, axis=-1) * (1 + ca) + sa
    x = x + ga * _A_ref(h, A_k, A_v, A_shift_k, A_ssm_k, A_ssm_D)
    h = _layernorm(x, axis=-1) * (1 + cb) + sb
    x = x + gb * _B_ref(h, B_w1, B_b1, B_w2, B_b2)
    return x
```

```cpp
#include <hip/hip_runtime.h>
#include <hip/hip_bf16.h>
#include <stdint.h>

typedef unsigned int u32;
typedef unsigned short u16;
typedef u32 v4u __attribute__((ext_vector_type(4)));
typedef u32 v2u __attribute__((ext_vector_type(2)));
typedef float f32x4 __attribute__((ext_vector_type(4)));
typedef float f32x2 __attribute__((ext_vector_type(2)));
typedef __bf16 bf16x8 __attribute__((ext_vector_type(8)));
typedef __bf16 bf16x2 __attribute__((ext_vector_type(2)));
#define LAS __attribute__((address_space(3)))

constexpr int S = 2048, D = 512, F = 1376;
constexpr int NOCT = F / 8;
constexpr int KS2 = F / 32;

constexpr size_t WS_KVR = 0;
constexpr size_t WS_XP = WS_KVR + (size_t)D * S * 2;
constexpr size_t WS_ST1 = WS_XP + (size_t)D * S * 2;
constexpr size_t WS_MODP = WS_ST1 + (size_t)S * 8;
constexpr size_t WS_MODF = WS_MODP + 4 * 3072 * 4;
constexpr size_t WS_W2T = WS_MODF + 3072 * 4;
constexpr size_t WS_W1T = WS_W2T + (size_t)8 * 43 * 4096;
constexpr size_t WS_COLP = WS_W1T + (size_t)16 * 16 * 6144;
constexpr size_t WS_X1O = WS_COLP + (size_t)8 * 16 * 192 * 4;
constexpr size_t WS_HO = WS_X1O + (size_t)D * S * 2;
constexpr size_t WS_END = WS_HO + (size_t)F * S * 2;

__device__ __forceinline__ float bf_lo(u32 v) { return __builtin_bit_cast(float, v << 16); }
__device__ __forceinline__ float bf_hi(u32 v) { return __builtin_bit_cast(float, v & 0xffff0000u); }
__device__ __forceinline__ float bf2f(u16 v) { return __builtin_bit_cast(float, (u32)v << 16); }
__device__ __forceinline__ u32 pack_bf2(float lo, float hi) {
  f32x2 v = {lo, hi};
  return __builtin_bit_cast(u32, __builtin_convertvector(v, bf16x2));
}
__device__ __forceinline__ float rbf(float x) { return bf_lo(pack_bf2(x, 0.f)); }
__device__ __forceinline__ f32x4 mfma16(v4u a, v4u b, f32x4 c) {
  return __builtin_amdgcn_mfma_f32_16x16x32_bf16(__builtin_bit_cast(bf16x8, a), __builtin_bit_cast(bf16x8, b), c, 0, 0, 0);
}
template <int CTRL> __device__ __forceinline__ float dpp_add(float v) {
  return v + __builtin_bit_cast(float, __builtin_amdgcn_update_dpp(0, __builtin_bit_cast(int, v), CTRL, 0xf, 0xf, true));
}
__device__ __forceinline__ float xor16_add(float v) {
  u32 u = __builtin_bit_cast(u32, v);
  auto r = __builtin_amdgcn_permlane16_swap(u, u, false, false);
  return __builtin_bit_cast(float, (u32)r[0]) + __builtin_bit_cast(float, (u32)r[1]);
}
__device__ __forceinline__ float xor32_add(float v) {
  u32 u = __builtin_bit_cast(u32, v);
  auto r = __builtin_amdgcn_permlane32_swap(u, u, false, false);
  return __builtin_bit_cast(float, (u32)r[0]) + __builtin_bit_cast(float, (u32)r[1]);
}
__device__ __forceinline__ float wave_sum(float v) {
  v = dpp_add<0xB1>(v);
  v = dpp_add<0x4E>(v);
  v = dpp_add<0x141>(v);
  v = dpp_add<0x140>(v);
  v = xor16_add(v);
  v = xor32_add(v);
  return v;
}
__device__ __forceinline__ int f0_of(int ct) { return ct <= 12 ? 88 * ct : 1056 + 80 * (ct - 12); }
__device__ __forceinline__ int noct_of(int ct) { return ct < 12 ? 11 : 10; }

__global__ void __launch_bounds__(256) k0_prep(const u16* __restrict__ x, const u16* __restrict__ k, const u16* __restrict__ v,
                                               const u16* __restrict__ shk, const u16* __restrict__ cvec, const u16* __restrict__ wmod,
                                               const u16* __restrict__ w2, unsigned char* __restrict__ ws) {
  __shared__ __attribute__((aligned(16))) u32 sm[2048];
  const int b = blockIdx.x, tid = threadIdx.x;
  if (b < 256) {
    const int cg = b & 7, rb = b >> 3;
    const int t0 = rb * 64, c0 = cg * 64;
    const int co = tid & 7, tq = tid >> 3;
    const int ta = t0 + 2 * tq;
    const int cb = c0 + 8 * co;
    v4u kr[5], vr[2], xr[2], sk[4];
#pragma unroll
    for (int i = 0; i < 5; ++i) {
      const int t = ta - 3 + i;
      kr[i] = t >= 0 ? *(const v4u*)(k + (size_t)t * D + cb) : v4u{0, 0, 0, 0};
    }
#pragma unroll
    for (int i = 0; i < 2; ++i) {
      vr[i] = *(const v4u*)(v + (size_t)(ta + i) * D + cb);
      xr[i] = *(const v4u*)(x + (size_t)(ta + i) * D + cb);
    }
#pragma unroll
    for (int i = 0; i < 4; ++i) sk[i] = *(const v4u*)(shk + (size_t)cb * 4 + 8 * i);
    u32* xp = (u32*)(ws + WS_XP);
#pragma unroll
    for (int q = 0; q < 4; ++q) {
      v2u o = {xr[0][q], xr[1][q]};
      *(v2u*)(xp + (size_t)(cb / 2 + q) * S + ta) = o;
    }
    const int dq = 31 - tq;
    const int col = (((dq >> 2) ^ co) << 2) + (dq & 3);
#pragma unroll
    for (int j = 0; j < 8; ++j) {
      const u32 s01 = sk[j >> 1][(j & 1) * 2], s23 = sk[j >> 1][(j & 1) * 2 + 1];
      const float s0 = bf_lo(s01), s1 = bf_hi(s01), s2 = bf_lo(s23), s3 = bf_hi(s23);
      float kk[5], vv[2];
#pragma unroll
      for (int i = 0; i < 5; ++i) kk[i] = (j & 1) ? bf_hi(kr[i][j >> 1]) : bf_lo(kr[i][j >> 1]);
#pragma unroll
      for (int i = 0; i < 2; ++i) vv[i] = (j & 1) ? bf_hi(vr[i][j >> 1]) : bf_lo(vr[i][j >> 1]);
      const float ks0 = kk[0] * s3 + kk[1] * s2 + kk[2] * s1 + kk[3] * s0;
      const float ks1 = kk[1] * s3 + kk[2] * s2 + kk[3] * s1 + kk[4] * s0;
      sm[(8 * co + j) * 32 + col] = pack_bf2(ks1 * vv[1], ks0 * vv[0]);
    }
    __syncthreads();
    u16* kvr = (u16*)(ws + WS_KVR);
#pragma unroll
    for (int i = 0; i < 2; ++i) {
      const int idx = tid + 256 * i, ch = idx >> 3, chunk = idx & 7;
      const v4u o = *(const v4u*)(sm + ch * 32 + ((chunk ^ (ch >> 3)) << 2));
      *(v4u*)(kvr + (size_t)(c0 + ch) * S + (1984 - t0) + 8 * chunk) = o;
    }
  } else if (b < 320) {
    const int lane = tid & 63, w = tid >> 6;
    const int r0 = (b - 256) * 32 + w * 8;
    v4u xr[8];
#pragma unroll
    for (int i = 0; i < 8; ++i) xr[i] = *(const v4u*)(x + (size_t)(r0 + i) * D + lane * 8);
    f32x2* st = (f32x2*)(ws + WS_ST1);
#pragma unroll
    for (int i = 0; i < 8; ++i) {
      float s = 0.f;
#pragma unroll
      for (int q = 0; q < 4; ++q) s += bf_lo(xr[i][q]) + bf_hi(xr[i][q]);
      const float m = wave_sum(s) * (1.f / D);
      float sq = 0.f;
#pragma unroll
      for (int q = 0; q < 4; ++q) {
        const float a = bf_lo(xr[i][q]) - m, c = bf_hi(xr[i][q]) - m;
        sq += a * a + c * c;
      }
      const float var = wave_sum(sq) * (1.f / D);
      const float den = rbf(sqrtf(rbf(rbf(var) + 1e-5f)));
      if (lane == 0) st[r0 + i] = f32x2{rbf(m), 1.f / den};
    }
  } else if (b < 512) {
    const int bb = b - 320, cbk = bb % 48, kq = bb / 48;
    const int chunk = tid & 7, r0 = tid >> 3;
    v4u wr[4];
    float sc[4];
#pragma unroll
    for (int i = 0; i < 4; ++i) {
      const int row = kq * 128 + r0 + 32 * i;
      wr[i] = *(const v4u*)(wmod + (size_t)row * 3072 + cbk * 64 + chunk * 8);
      const float cv = bf2f(cvec[row]);
      sc[i] = rbf(cv / (1.f + __expf(-cv)));
    }
    float acc[8];
#pragma unroll
    for (int j = 0; j < 8; ++j) {
      float a = 0.f;
#pragma unroll
      for (int i = 0; i < 4; ++i) a += sc[i] * ((j & 1) ? bf_hi(wr[i][j >> 1]) : bf_lo(wr[i][j >> 1]));
      a = dpp_add<0x128>(a);
      a = xor16_add(a);
      a = xor32_add(a);
      acc[j] = a;
    }
    float* sf = (float*)sm;
    const int lane = tid & 63, w = tid >> 6;
    if (lane < 8) {
#pragma unroll
      for (int j = 0; j < 8; ++j) sf[w * 64 + lane * 8 + j] = acc[j];
    }
    __syncthreads();
    if (tid < 64) {
      float* mp = (float*)(ws + WS_MODP);
      mp[kq * 3072 + cbk * 64 + tid] = (sf[tid] + sf[64 + tid]) + (sf[128 + tid] + sf[192 + tid]);
    }
  } else {
    const int gt = (b - 512) * 256 + tid;
    const int dq = gt & 127, fo = gt >> 7;
    v2u r[8];
#pragma unroll
    for (int jj = 0; jj < 8; ++jj) r[jj] = *(const v2u*)(w2 + (size_t)(8 * fo + jj) * D + 4 * dq);
    const int d = 4 * dq, ct = d >> 6, dl = d & 63;
    const int j = 2 * (dl >> 5) + ((dl >> 2) & 1), cq = (dl >> 3) & 3;
    const int ks = fo >> 2, g = fo & 3;
    v4u* dst = (v4u*)(ws + WS_W2T) + ((size_t)(ct * KS2 + ks) * 4 + j) * 64 + g * 16 + 4 * cq;
#pragma unroll
    for (int i = 0; i < 4; ++i) {
      v4u o;
#pragma unroll
      for (int p = 0; p < 4; ++p)
        o[p] = __builtin_amdgcn_perm(r[2 * p + 1][i >> 1], r[2 * p][i >> 1], (i & 1) ? 0x07060302u : 0x05040100u);
      dst[i] = o;
    }
  }
}

constexpr int C_HCH = 64 + 4096;
constexpr int C_UCOPY = 4544 + 32;
constexpr int C_UCH = 2 * C_UCOPY;
constexpr int C_HOFF = 0, C_UOFF = 2 * C_HCH, C_ROFF = C_UOFF + 2 * C_UCH;
constexpr int C_LDS = C_ROFF + 32768;
static_assert(C_UOFF % 16 == 0 && C_ROFF % 16 == 0, "align");

__global__ void __launch_bounds__(256) k1_conv(const u16* __restrict__ ssmk, const u16* __restrict__ ssmD, const u16* __restrict__ w1,
                                               unsigned char* __restrict__ ws) {
  __shared__ __attribute__((aligned(16))) unsigned char smem[C_LDS];
  const int b = blockIdx.x, tid = threadIdx.x, lane = tid & 63;
  const int w = __builtin_amdgcn_readfirstlane(tid >> 6);
  const float* modp = (const float*)(ws + WS_MODP);
  if (b < 256) {
    const int pair = (b & 7) * 32 + (b >> 3), c0 = 2 * pair;
    const u16* kvr = (const u16*)(ws + WS_KVR);
    v4u hv[2], uv[2];
    u32 un[2];
#pragma unroll
    for (int c = 0; c < 2; ++c) {
      uv[c] = *(const v4u*)(kvr + (size_t)(c0 + c) * S + tid * 8);
      un[c] = tid < 255 ? *(const u32*)(kvr + (size_t)(c0 + c) * S + tid * 8 + 8) : 0u;
      hv[c] = *(const v4u*)(ssmk + (size_t)(c0 + c) * S + tid * 8);
    }
    const int col = lane & 15, g = lane >> 4;
    const int tb = 512 * w + 128 * (g >> 1) + 2 * col + (g & 1);
    u32 xv[8];
    f32x2 stv[8];
    const u32* xp = (const u32*)(ws + WS_XP) + (size_t)pair * S;
    const f32x2* st = (const f32x2*)(ws + WS_ST1);
#pragma unroll
    for (int i = 0; i < 8; ++i) {
      const int t = tb + 256 * (i >> 2) + 32 * (i & 3);
      xv[i] = xp[t];
      stv[i] = st[t];
    }
    float md[6][2];
#pragma unroll
    for (int q = 0; q < 6; ++q)
#pragma unroll
      for (int c = 0; c < 2; ++c) {
        const int j = q * 512 + c0 + c;
        md[q][c] = rbf((modp[j] + modp[3072 + j]) + (modp[6144 + j] + modp[9216 + j]));
      }
    if (tid < 2) {
      float* modf = (float*)(ws + WS_MODF);
#pragma unroll
      for (int q = 0; q < 6; ++q) modf[q * 512 + c0 + tid] = tid ? md[q][1] : md[q][0];
    }
    const u32 dd = *(const u32*)(ssmD + c0);
    if (tid < 8) *(v4u*)(smem + C_HOFF + (tid >> 2) * C_HCH + (tid & 3) * 16) = v4u{0, 0, 0, 0};
    if (tid >= 128 && tid < 128 + 112) {
      const int z = tid - 128, cp = z / 28, ck = z % 28;
      *(v4u*)(smem + C_UOFF + cp * C_UCOPY + 4096 + ck * 16) = v4u{0, 0, 0, 0};
    }
#pragma unroll
    for (int c = 0; c < 2; ++c) {
      *(v4u*)(smem + C_HOFF + c * C_HCH + 64 + tid * 16) = hv[c];
      *(v4u*)(smem + C_UOFF + c * C_UCH + C_UCOPY + tid * 16) = uv[c];
      v4u s;
      s[0] = __builtin_amdgcn_alignbit(uv[c][1], uv[c][0], 16);
      s[1] = __builtin_amdgcn_alignbit(uv[c][2], uv[c][1], 16);
      s[2] = __builtin_amdgcn_alignbit(uv[c][3], uv[c][2], 16);
      s[3] = __builtin_amdgcn_alignbit(un[c], uv[c][3], 16);
      *(v4u*)(smem + C_UOFF + c * C_UCH + tid * 16) = s;
    }
    __syncthreads();
    const int ch = w >> 1, hf = w & 1;
    const int row = col;
    const int e = (row >> 2) & 1, y = (row & 3) + 4 * (row >> 3);
    const u32* hb = (const u32*)(smem + C_HOFF + ch * C_HCH) + (col + 4 * g + 1 + 64 * hf);
    const unsigned char* ub = smem + C_UOFF + ch * C_UCH + e * C_UCOPY + 2 * (2016 - 1792 - 32 * y + 8 * g) + 256 * hf;
    f32x4 acc[8];
#pragma unroll
    for (int n = 0; n < 8; ++n) acc[n] = f32x4{0.f, 0.f, 0.f, 0.f};
#pragma unroll
    for (int rr = 0; rr < 4; ++rr) {
      v4u hfr[8], ufr[8];
#pragma unroll
      for (int s = 0; s < 8; ++s) {
        ufr[s] = *(const v4u*)(ub + 64 * rr + 512 * (7 - s));
#pragma unroll
        for (int jj = 0; jj < 4; ++jj) hfr[s][jj] = hb[16 * rr + 128 * s + jj];
      }
#pragma unroll
      for (int n = 0; n < 8; ++n)
#pragma unroll
        for (int s = 0; s <= n; ++s) acc[n] = mfma16(ufr[n - s], hfr[s], acc[n]);
    }
    f32x4* red = (f32x4*)(smem + C_ROFF);
#pragma unroll
    for (int n = 0; n < 8; ++n) red[(w * 8 + n) * 64 + lane] = acc[n];
    __syncthreads();
    const float d0 = bf_lo(dd), d1 = bf_hi(dd);
    const float ca0 = rbf(1.f + md[1][0]), ca1 = rbf(1.f + md[1][1]);
    u32* x1o = (u32*)(ws + WS_X1O) + (size_t)(c0 >> 3) * S * 4 + ((c0 & 7) >> 1);
    const u16* u1a = (const u16*)(smem + C_UOFF + C_UCOPY);
    const u16* u1b = (const u16*)(smem + C_UOFF + C_UCH + C_UCOPY);
#pragma unroll
    for (int i = 0; i < 2; ++i) {
      const int n = 2 * w + i;
      const f32x4 y0 = red[(0 * 8 + n) * 64 + lane] + red[(1 * 8 + n) * 64 + lane];
      const f32x4 y1 = red[(2 * 8 + n) * 64 + lane] + red[(3 * 8 + n) * 64 + lane];
#pragma unroll
      for (int r = 0; r < 4; ++r) {
        const int t = tb + 256 * i + 32 * r;
        const float kv0 = bf2f(u1a[2047 - t]), kv1 = bf2f(u1b[2047 - t]);
        const float x0 = bf_lo(xv[4 * i + r]), x1 = bf_hi(xv[4 * i + r]);
        const f32x2 sv = stv[4 * i + r];
        const float z0 = y0[r] + kv0 * d0, z1 = y1[r] + kv1 * d1;
        const float h0 = (x0 - sv[0]) * sv[1] * ca0 + md[0][0];
        const float h1 = (x1 - sv[0]) * sv[1] * ca1 + md[0][1];
        const float o0 = x0 + md[2][0] * (h0 * z0), o1 = x1 + md[2][1] * (h1 * z1);
        x1o[(size_t)t * 4] = pack_bf2(o0, o1);
      }
    }
  } else {
    const int bb = b - 256, ct = bb & 15, ke = bb >> 4;
    const int q = tid % 24, ko = tid / 24;
    const int slot = q >> 1;
    const bool act = tid < 192;
    const bool valid = act && slot < noct_of(ct);
    const int k0 = 64 * ke + 8 * ko;
    v2u r[8];
#pragma unroll
    for (int jj = 0; jj < 8; ++jj) r[jj] = valid ? *(const v2u*)(w1 + (size_t)(k0 + jj) * F + f0_of(ct) + 4 * q) : v2u{0, 0};
    float* sf = (float*)smem;
    if (tid < 128) {
      const int which = tid >> 6, kk = tid & 63;
      const int j = (which ? 3 : 4) * 512 + 64 * ke + kk;
      const float m = rbf((modp[j] + modp[3072 + j]) + (modp[6144 + j] + modp[9216 + j]));
      sf[tid] = which ? m : rbf(1.f + m);
    }
    __syncthreads();
    float* pp = sf + 128;
    if (act) {
      float pu[4] = {0.f, 0.f, 0.f, 0.f}, ps[4] = {0.f, 0.f, 0.f, 0.f};
      v2u sr[8];
#pragma unroll
      for (int jj = 0; jj < 8; ++jj) {
        const float sc = sf[8 * ko + jj], sh = sf[64 + 8 * ko + jj];
        const float a0 = bf_lo(r[jj][0]), a1 = bf_hi(r[jj][0]), a2 = bf_lo(r[jj][1]), a3 = bf_hi(r[jj][1]);
        sr[jj][0] = pack_bf2(a0 * sc, a1 * sc);
        sr[jj][1] = pack_bf2(a2 * sc, a3 * sc);
        pu[0] += bf_lo(sr[jj][0]);
        pu[1] += bf_hi(sr[jj][0]);
        pu[2] += bf_lo(sr[jj][1]);
        pu[3] += bf_hi(sr[jj][1]);
        ps[0] += a0 * sh;
        ps[1] += a1 * sh;
        ps[2] += a2 * sh;
        ps[3] += a3 * sh;
      }
      const int j = 2 * (slot >> 2) + (q & 1), cq = slot & 3;
      const int ks = 2 * ke + (ko >> 2), g = ko & 3;
      v4u* dst = (v4u*)(ws + WS_W1T) + ((size_t)(ct * 16 + ks) * 6 + j) * 64 + g * 16 + 4 * cq;
#pragma unroll
      for (int i = 0; i < 4; ++i) {
        v4u o;
#pragma unroll
        for (int p = 0; p < 4; ++p)
          o[p] = __builtin_amdgcn_perm(sr[2 * p + 1][i >> 1], sr[2 * p][i >> 1], (i & 1) ? 0x07060302u : 0x05040100u);
        if (slot == 11) o = v4u{0x3f803f80u, 0x3f803f80u, 0x3f803f80u, 0x3f803f80u};
        dst[i] = o;
      }
      *(f32x4*)(pp + ko * 96 + 4 * q) = f32x4{pu[0], pu[1], pu[2], pu[3]};
      *(f32x4*)(pp + 768 + ko * 96 + 4 * q) = f32x4{ps[0], ps[1], ps[2], ps[3]};
    }
    __syncthreads();
    if (act) {
      const int which = tid / 96, fc = tid % 96;
      float a = 0.f;
#pragma unroll
      for (int i = 0; i < 8; ++i) a += pp[which * 768 + i * 96 + fc];
      ((float*)(ws + WS_COLP))[(ke * 16 + ct) * 192 + tid] = a;
    }
  }
}

constexpr int G1_LDS = 98304 + 192 * 4;
__device__ __forceinline__ void glds16(const void* gsrc, u32 lds_dst) {
  u32 keep;
  asm volatile("s_mov_b32 %0, m0\n\ts_mov_b32 m0, %2\n\ts_nop 0\n\tglobal_load_lds_dwordx4 %1, off\n\ts_mov_b32 m0, %0"
               : "=&s"(keep)
               : "v"(gsrc), "s"(lds_dst)
               : "memory");
}

__global__ void __launch_bounds__(256, 1) k2_gemm1(const u16* __restrict__ b1, unsigned char* __restrict__ ws) {
  extern __shared__ __attribute__((aligned(16))) unsigned char smem[];
  const int b = blockIdx.x, tid = threadIdx.x, lane = tid & 63;
  const int w = __builtin_amdgcn_readfirstlane(tid >> 6);
  const int idx = b >> 3, rp = 2 * (b & 7) + (idx >> 4), ct = idx & 15;
  const int c = lane & 15, g = lane >> 4;
  const u32 lbase = (u32)(uintptr_t)(LAS unsigned char*)smem;
  const int f0 = f0_of(ct), noct = noct_of(ct);
  float cpv[8];
  u16 b1v;
  {
    const int tc = tid < 192 ? tid : 191;
    const float* cp = (const float*)(ws + WS_COLP) + ct * 192 + tc;
#pragma unroll
    for (int i = 0; i < 8; ++i) cpv[i] = cp[i * 16 * 192];
    const int fb = (tid >= 96 && tid - 96 < 8 * noct) ? tid - 96 : 0;
    b1v = b1[f0 + fb];
  }
  __builtin_amdgcn_sched_barrier(0);
  {
    const unsigned char* src = ws + WS_W1T + (size_t)ct * 98304 + w * 1024 + lane * 16;
#pragma unroll
    for (int i = 0; i < 24; ++i) glds16(src + i * 4096, lbase + w * 1024 + i * 4096);
  }
  const int t0 = 128 * rp + 32 * w;
  const v4u* xo = (const v4u*)(ws + WS_X1O) + (size_t)g * S + t0 + c;
  v4u xf[16][2];
#pragma unroll
  for (int ks = 0; ks < 16; ++ks)
#pragma unroll
    for (int tt = 0; tt < 2; ++tt) xf[ks][tt] = xo[(size_t)ks * 4 * S + 16 * tt];
  __builtin_amdgcn_sched_barrier(0);
  asm volatile("s_waitcnt vmcnt(32)" ::: "memory");
  __builtin_amdgcn_s_barrier();
  __builtin_amdgcn_sched_barrier(0);
  f32x4 acc[6][2], sq[2];
#pragma unroll
  for (int tt = 0; tt < 2; ++tt) {
    sq[tt] = f32x4{0.f, 0.f, 0.f, 0.f};
#pragma unroll
    for (int j = 0; j < 6; ++j) acc[j][tt] = f32x4{0.f, 0.f, 0.f, 0.f};
  }
  const v4u* wl = (const v4u*)smem + lane;
#pragma unroll
  for (int ks = 0; ks < 16; ++ks) {
    v4u wf[6];
#pragma unroll
    for (int j = 0; j < 6; ++j) wf[j] = wl[(ks * 6 + j) * 64];
#pragma unroll
    for (int tt = 0; tt < 2; ++tt) {
#pragma unroll
      for (int j = 0; j < 6; ++j) acc[j][tt] = mfma16(wf[j], xf[ks][tt], acc[j][tt]);
      sq[tt] = mfma16(xf[ks][tt], xf[ks][tt], sq[tt]);
    }
  }
  float* cs = (float*)(smem + 98304);
  {
    float cv = ((cpv[0] + cpv[1]) + (cpv[2] + cpv[3])) + ((cpv[4] + cpv[5]) + (cpv[6] + cpv[7]));
    if (tid >= 96) cv += bf2f(b1v);
    if (tid < 192) cs[tid] = cv;
  }
  __syncthreads();
  v4u* ho = (v4u*)(ws + WS_HO) + (size_t)(f0 >> 3) * S;
#pragma unroll
  for (int tt = 0; tt < 2; ++tt) {
    const float sum = __shfl(acc[4][tt][0], 48 + c);
    const int r = c & 3;
    const float dsel = r == 0 ? sq[tt][0] : r == 1 ? sq[tt][1] : r == 2 ? sq[tt][2] : sq[tt][3];
    const float ssq = __shfl(dsel, c + 16 * (c >> 2));
    const float m = sum * (1.f / D);
    const float var = fmaxf(ssq * (1.f / D) - m * m, 0.f);
    const float rs = 1.f / rbf(sqrtf(rbf(rbf(var) + 1e-5f)));
    const float mr = -rbf(m) * rs;
#pragma unroll
    for (int jh = 0; jh < 3; ++jh) {
      const int slot = 4 * jh + g;
      v4u o;
#pragma unroll
      for (int jl = 0; jl < 2; ++jl) {
        const f32x4 u1 = *(const f32x4*)(cs + 8 * slot + 4 * jl), s1 = *(const f32x4*)(cs + 96 + 8 * slot + 4 * jl);
        float hv[4];
#pragma unroll
        for (int q = 0; q < 4; ++q) {
          float a = acc[2 * jh + jl][tt][q] * rs + (mr * u1[q] + s1[q]);
          a = fmaxf(a, 0.f);
          hv[q] = a * a;
        }
        o[2 * jl] = pack_bf2(hv[0], hv[1]);
        o[2 * jl + 1] = pack_bf2(hv[2], hv[3]);
      }
      if (slot < noct) ho[(size_t)slot * S + t0 + 16 * tt + c] = o;
    }
  }
}

__global__ void __launch_bounds__(256, 1) k3_gemm2(const u16* __restrict__ b2, unsigned char* __restrict__ ws, u16* __restrict__ out) {
  __shared__ __attribute__((aligned(16))) f32x4 red[4 * 16 * 64];
  const int b = blockIdx.x, tid = threadIdx.x, lane = tid & 63;
  const int w = __builtin_amdgcn_readfirstlane(tid >> 6);
  const int idx = b >> 3, rt = 4 * (b & 7) + (idx >> 3), ct = idx & 7;
  const int c = lane & 15, g = lane >> 4;
  const int t0 = 64 * rt;
  const int ks0 = 11 * w;
  const v4u* hp = (const v4u*)(ws + WS_HO) + (size_t)(4 * ks0 + g) * S + t0 + c;
  const v4u* wp = (const v4u*)(ws + WS_W2T) + (size_t)(ct * KS2 + ks0) * 256 + lane;
  constexpr int W = 5;
  v4u hf[W][4], wf[W][4];
#pragma unroll
  for (int i = 0; i < W; ++i)
#pragma unroll
    for (int q = 0; q < 4; ++q) {
      wf[i][q] = wp[i * 256 + q * 64];
      hf[i][q] = hp[(size_t)i * 4 * S + 16 * q];
    }
  const int te = t0 + 16 * w + c;
  v4u x1v[2], b2v[2];
  f32x4 gbv[4];
#pragma unroll
  for (int h = 0; h < 2; ++h) {
    const int oct = 8 * ct + 4 * h + g;
    x1v[h] = ((const v4u*)(ws + WS_X1O))[(size_t)oct * S + te];
    b2v[h] = *(const v4u*)(b2 + 8 * oct);
    gbv[2 * h] = *(const f32x4*)((const float*)(ws + WS_MODF) + 5 * 512 + 8 * oct);
    gbv[2 * h + 1] = *(const f32x4*)((const float*)(ws + WS_MODF) + 5 * 512 + 8 * oct + 4);
  }
  __builtin_amdgcn_sched_barrier(0);
  f32x4 acc[4][4];
#pragma unroll
  for (int j = 0; j < 4; ++j)
#pragma unroll
    for (int tt = 0; tt < 4; ++tt) acc[j][tt] = f32x4{0.f, 0.f, 0.f, 0.f};
  const int nks = w < 3 ? 11 : 10;
#pragma unroll
  for (int i = 0; i < 11; ++i) {
    if (i < nks) {
      v4u ch[4], cw[4];
#pragma unroll
      for (int q = 0; q < 4; ++q) {
        ch[q] = hf[i % W][q];
        cw[q] = wf[i % W][q];
      }
#pragma unroll
      for (int j = 0; j < 4; ++j)
#pragma unroll
        for (int tt = 0; tt < 4; ++tt) acc[j][tt] = mfma16(cw[j], ch[tt], acc[j][tt]);
      if (i + W < 11 && i + W < nks) {
#pragma unroll
        for (int q = 0; q < 4; ++q) {
          wf[i % W][q] = wp[(i + W) * 256 + q * 64];
          hf[i % W][q] = hp[(size_t)(i + W) * 4 * S + 16 * q];
        }
      }
      __builtin_amdgcn_sched_barrier(0);
    }
  }
#pragma unroll
  for (int j = 0; j < 4; ++j)
#pragma unroll
    for (int tt = 0; tt < 4; ++tt) red[((w * 4 + tt) * 4 + j) * 64 + lane] = acc[j][tt];
  __syncthreads();
#pragma unroll
  for (int h = 0; h < 2; ++h) {
    v4u o;
#pragma unroll
    for (int jl = 0; jl < 2; ++jl) {
      const int j = 2 * h + jl;
      f32x4 a = (red[((0 * 4 + w) * 4 + j) * 64 + lane] + red[((1 * 4 + w) * 4 + j) * 64 + lane]) +
                (red[((2 * 4 + w) * 4 + j) * 64 + lane] + red[((3 * 4 + w) * 4 + j) * 64 + lane]);
      const f32x4 gb = gbv[j];
      const u32 xa = x1v[h][2 * jl], xb = x1v[h][2 * jl + 1], ba = b2v[h][2 * jl], bb = b2v[h][2 * jl + 1];
      const float o0 = bf_lo(xa) + gb[0] * (a[0] + bf_lo(ba));
      const float o1 = bf_hi(xa) + gb[1] * (a[1] + bf_hi(ba));
      const float o2 = bf_lo(xb) + gb[2] * (a[2] + bf_lo(bb));
      const float o3 = bf_hi(xb) + gb[3] * (a[3] + bf_hi(bb));
      o[2 * jl] = pack_bf2(o0, o1);
      o[2 * jl + 1] = pack_bf2(o2, o3);
    }
    *(v4u*)(out + (size_t)te * D + 64 * ct + 32 * h + 8 * g) = o;
  }
}

extern "C" void kernel_launch(void* const* d_in, const int* in_sizes, int n_in, void* d_out, int out_size, void* d_ws, size_t ws_size,
                              hipStream_t stream) {
  const u16* q = (const u16*)d_in[0];
  const u16* k = (const u16*)d_in[1];
  const u16* v = (const u16*)d_in[2];
  const u16* shk = (const u16*)d_in[3];
  const u16* ssmk = (const u16*)d_in[4];
  const u16* ssmD = (const u16*)d_in[5];
  const u16* w1 = (const u16*)d_in[7];
  const u16* b1 = (const u16*)d_in[8];
  const u16* w2 = (const u16*)d_in[9];
  const u16* b2 = (const u16*)d_in[10];
  const u16* cvec = (const u16*)d_in[11];
  const u16* wmod = (const u16*)d_in[12];
  unsigned char* ws = (unsigned char*)d_ws;
  (void)hipFuncSetAttribute((const void*)k2_gemm1, hipFuncAttributeMaxDynamicSharedMemorySize, G1_LDS);
  k0_prep<<<598, 256, 0, stream>>>(q, k, v, shk, cvec, wmod, w2, ws);
  k1_conv<<<384, 256, 0, stream>>>(ssmk, ssmD, w1, ws);
  k2_gemm1<<<256, 256, G1_LDS, stream>>>(b1, ws);
  k3_gemm2<<<256, 256, 0, stream>>>(b2, ws, (u16*)d_out);
}
```

```cpp
#include <hip/hip_runtime.h>
#include <hip/hip_bf16.h>
#include <stdint.h>

typedef unsigned int u32;
typedef unsigned short u16;
typedef u32 v4u __attribute__((ext_vector_type(4)));
typedef u32 v2u __attribute__((ext_vector_type(2)));
typedef float f32x4 __attribute__((ext_vector_type(4)));
typedef float f32x2 __attribute__((ext_vector_type(2)));
typedef __bf16 bf16x8 __attribute__((ext_vector_type(8)));
typedef __bf16 bf16x2 __attribute__((ext_vector_type(2)));
typedef short v4s __attribute__((ext_vector_type(4)));
#define LAS __attribute__((address_space(3)))
#define GAS __attribute__((address_space(1)))

constexpr int S = 2048, D = 512, F = 1376;

constexpr int WS_EPOCH = 0;
constexpr int WS_FLAG0 = 4096;
constexpr int WS_FLAG2 = WS_FLAG0 + 256 * 128;
constexpr int WS_CNT1 = WS_FLAG2 + 256 * 128;
constexpr int WS_ST1 = WS_CNT1 + 256 * 256;
constexpr int WS_FIN = WS_ST1 + S * 8;
constexpr int WS_MODP = WS_FIN + D * 16;
constexpr int WS_KVR = 1 << 20;
constexpr int WS_XP = WS_KVR + D * S * 2;
constexpr int WS_X1P = WS_XP + D * S * 2;
constexpr int WS_HO = WS_X1P + D * S * 2;
constexpr int WS_END = WS_HO + F * S * 2;
static_assert(WS_MODP + 4 * 3072 * 4 <= WS_KVR, "ctrl region");

constexpr int L_W1 = 0;
constexpr int C_HCH = 64 + 4096;
constexpr int C_UCOPY = 4544 + 32;
constexpr int C_UCH = 2 * C_UCOPY;
constexpr int L_H = 98304, L_U = L_H + 2 * C_HCH, L_RED = L_U + 2 * C_UCH, L_MISC = L_RED + 32768;
constexpr int L_SA = L_MISC, L_SB = L_SA + 1024, L_GB = L_SB + 1024, L_END = L_GB + 1024;
constexpr int L_CSP = L_H, L_CS = L_H + 4 * 192 * 4;
static_assert(L_U % 16 == 0 && L_RED % 16 == 0 && L_END <= 163840, "lds");

constexpr int W2_A = 2, W2_C = 6, W2_E = 3;
static_assert(W2_A + W2_C + W2_E == 11, "w2 steps");
constexpr u32 SPIN_CAP = 1u << 16;

#define STAMP(i)

__device__ __forceinline__ float bf_lo(u32 v) { return __builtin_bit_cast(float, v << 16); }
__device__ __forceinline__ float bf_hi(u32 v) { return __builtin_bit_cast(float, v & 0xffff0000u); }
__device__ __forceinline__ float bf2f(u16 v) { return __builtin_bit_cast(float, (u32)v << 16); }
__device__ __forceinline__ u32 pack_bf2(float lo, float hi) {
  f32x2 v = {lo, hi};
  return __builtin_bit_cast(u32, __builtin_convertvector(v, bf16x2));
}
__device__ __forceinline__ float rbf(float x) { return bf_lo(pack_bf2(x, 0.f)); }
__device__ __forceinline__ f32x4 mfma16(v4u a, v4u b, f32x4 c) {
  return __builtin_amdgcn_mfma_f32_16x16x32_bf16(__builtin_bit_cast(bf16x8, a), __builtin_bit_cast(bf16x8, b), c, 0, 0, 0);
}
template <int CTRL> __device__ __forceinline__ float dpp_add(float v) {
  return v + __builtin_bit_cast(float, __builtin_amdgcn_update_dpp(0, __builtin_bit_cast(int, v), CTRL, 0xf, 0xf, true));
}
__device__ __forceinline__ float xor16_add(float v) {
  u32 u = __builtin_bit_cast(u32, v);
  auto r = __builtin_amdgcn_permlane16_swap(u, u, false, false);
  return __builtin_bit_cast(float, (u32)r[0]) + __builtin_bit_cast(float, (u32)r[1]);
}
__device__ __forceinline__ float xor32_add(float v) {
  u32 u = __builtin_bit_cast(u32, v);
  auto r = __builtin_amdgcn_permlane32_swap(u, u, false, false);
  return __builtin_bit_cast(float, (u32)r[0]) + __builtin_bit_cast(float, (u32)r[1]);
}
__device__ __forceinline__ float wave_sum(float v) {
  v = dpp_add<0xB1>(v);
  v = dpp_add<0x4E>(v);
  v = dpp_add<0x141>(v);
  v = dpp_add<0x140>(v);
  v = xor16_add(v);
  v = xor32_add(v);
  return v;
}
__device__ __forceinline__ float rdlane(float v, int l) { return __builtin_bit_cast(float, __builtin_amdgcn_readlane(__builtin_bit_cast(int, v), l)); }
__device__ __forceinline__ v4u ldnt(const u16* p) { return __builtin_nontemporal_load((const v4u*)p); }
__device__ __forceinline__ void glds16(const void* gsrc, u32 lds_dst) {
  u32 keep;
  asm volatile("s_mov_b32 %0, m0\n\ts_mov_b32 m0, %2\n\ts_nop 0\n\tglobal_load_lds_dwordx4 %1, off\n\ts_mov_b32 m0, %0"
               : "=&s"(keep)
               : "v"(gsrc), "s"(lds_dst));
}
#define SC1 16
#define LD128(off) __builtin_amdgcn_raw_buffer_load_b128(rs, (off), 0, SC1)
#define LD64(off) __builtin_amdgcn_raw_buffer_load_b64(rs, (off), 0, SC1)
#define LD32(off) __builtin_amdgcn_raw_buffer_load_b32(rs, (off), 0, SC1)
#define ST128(v, off) __builtin_amdgcn_raw_buffer_store_b128((v), rs, (off), 0, SC1)
#define ST64(v, off) __builtin_amdgcn_raw_buffer_store_b64((v), rs, (off), 0, SC1)
#define ST32(v, off) __builtin_amdgcn_raw_buffer_store_b32((v), rs, (off), 0, SC1)
#define DRAIN() asm volatile("s_waitcnt vmcnt(0)" ::: "memory")
#define CFENCE() asm volatile("" ::: "memory")
#define BAR() do { asm volatile("s_waitcnt lgkmcnt(0)" ::: "memory"); __builtin_amdgcn_s_barrier(); asm volatile("" ::: "memory"); } while (0)

__global__ void __launch_bounds__(256, 1)
    mega(const u16* __restrict__ x, const u16* __restrict__ k, const u16* __restrict__ v, const u16* __restrict__ shk,
         const u16* __restrict__ ssmk, const u16* __restrict__ ssmD, const u16* __restrict__ w1, const u16* __restrict__ b1,
         const u16* __restrict__ w2, const u16* __restrict__ b2, const u16* __restrict__ cvec, const u16* __restrict__ wmod,
         unsigned char* ws, u16* __restrict__ out) {
  extern __shared__ __attribute__((aligned(16))) unsigned char smem[];
  const int b = blockIdx.x, tid = threadIdx.x, lane = tid & 63;
  const int w = __builtin_amdgcn_readfirstlane(tid >> 6);
  const int grp = b & 7, mem = b >> 3;
  const int c = lane & 15, g = lane >> 4;
  const __amdgpu_buffer_rsrc_t rs = __builtin_amdgcn_make_buffer_rsrc((void*)ws, 0, WS_END, 0x00020000);
  STAMP(0);
  const u32 epoch_v = LD32(WS_EPOCH);
  if (tid == 0) ST32(0u, WS_CNT1 + b * 256);

  const int pair = grp * 32 + mem, c0 = 2 * pair;
  v4u hv[2];
  u32 st_pay[2];
  {
    const int t0 = mem * 64, cb0 = grp * 64;
    const int co = tid & 7, tq = tid >> 3;
    const int ta = t0 + 2 * tq;
    const int cb = cb0 + 8 * co;
    v4u kr[5], vr[2], xr[2], sk[4], wr[4], sr[2];
    u16 cr[4];
#pragma unroll
    for (int i = 0; i < 5; ++i) {
      const int t = ta - 3 + i;
      kr[i] = *(const v4u*)(k + (size_t)(t >= 0 ? t : 0) * D + cb);
    }
#pragma unroll
    for (int i = 0; i < 2; ++i) vr[i] = ldnt(v + (size_t)(ta + i) * D + cb);
#pragma unroll
    for (int i = 0; i < 4; ++i) sk[i] = *(const v4u*)(shk + (size_t)cb * 4 + 8 * i);
#pragma unroll
    for (int i = 0; i < 2; ++i) xr[i] = ldnt(x + (size_t)(ta + i) * D + cb);
    const int mq = mem % 6, kq = mem < 24 ? mem / 6 : 0;
#pragma unroll
    for (int i = 0; i < 4; ++i) {
      const int row = kq * 128 + tq + 32 * i;
      if (mem < 24) wr[i] = ldnt(wmod + (size_t)row * 3072 + mq * 512 + cb);
      cr[i] = cvec[row];
    }
#pragma unroll
    for (int i = 0; i < 2; ++i) sr[i] = ldnt(x + (size_t)(8 * b + 2 * w + i) * D + lane * 8);
#pragma unroll
    for (int i = 0; i < 2; ++i) hv[i] = ldnt(ssmk + (size_t)(c0 + i) * S + tid * 8);
    __builtin_amdgcn_sched_barrier(0);
    if (tid < 8) *(v4u*)(smem + L_H + (tid >> 2) * C_HCH + (tid & 3) * 16) = v4u{0, 0, 0, 0};
    if (tid >= 128 && tid < 128 + 112) {
      const int z = tid - 128, cp = z / 28, ck = z % 28;
      *(v4u*)(smem + L_U + cp * C_UCOPY + 4096 + ck * 16) = v4u{0, 0, 0, 0};
    }
#pragma unroll
    for (int i = 0; i < 3; ++i)
      if (ta - 3 + i < 0) kr[i] = v4u{0, 0, 0, 0};
    u32* sm = (u32*)(smem + L_RED);
    const int dq = 31 - tq;
    const int col = (((dq >> 2) ^ co) << 2) + (dq & 3);
#pragma unroll
    for (int j = 0; j < 8; ++j) {
      const u32 s01 = sk[j >> 1][(j & 1) * 2], s23 = sk[j >> 1][(j & 1) * 2 + 1];
      const float s0 = bf_lo(s01), s1 = bf_hi(s01), s2 = bf_lo(s23), s3 = bf_hi(s23);
      float kk[5], vv[2];
#pragma unroll
      for (int i = 0; i < 5; ++i) kk[i] = (j & 1) ? bf_hi(kr[i][j >> 1]) : bf_lo(kr[i][j >> 1]);
#pragma unroll
      for (int i = 0; i < 2; ++i) vv[i] = (j & 1) ? bf_hi(vr[i][j >> 1]) : bf_lo(vr[i][j >> 1]);
      const float ks0 = kk[0] * s3 + kk[1] * s2 + kk[2] * s1 + kk[3] * s0;
      const float ks1 = kk[1] * s3 + kk[2] * s2 + kk[3] * s1 + kk[4] * s0;
      sm[(8 * co + j) * 32 + col] = pack_bf2(ks1 * vv[1], ks0 * vv[0]);
    }
    STAMP(1);
#pragma unroll
    for (int q = 0; q < 4; ++q) ST64((v2u{xr[0][q], xr[1][q]}), WS_XP + ((cb / 2 + q) * S + ta) * 4);
    float* sf = (float*)(smem + L_RED + 8192);
    if (mem < 24) {
      float sc[4];
#pragma unroll
      for (int i = 0; i < 4; ++i) {
        const float cv = bf2f(cr[i]);
        sc[i] = rbf(cv / (1.f + __expf(-cv)));
      }
      float acc[8];
#pragma unroll
      for (int j = 0; j < 8; ++j) {
        float a = 0.f;
#pragma unroll
        for (int i = 0; i < 4; ++i) a += sc[i] * ((j & 1) ? bf_hi(wr[i][j >> 1]) : bf_lo(wr[i][j >> 1]));
        a = dpp_add<0x128>(a);
        a = xor16_add(a);
        a = xor32_add(a);
        acc[j] = a;
      }
      if (lane < 8) {
        *(f32x4*)(sf + w * 64 + lane * 8) = f32x4{acc[0], acc[1], acc[2], acc[3]};
        *(f32x4*)(sf + w * 64 + lane * 8 + 4) = f32x4{acc[4], acc[5], acc[6], acc[7]};
      }
    }
    BAR();
#pragma unroll
    for (int i = 0; i < 2; ++i) {
      const int idx = tid + 256 * i, ch = idx >> 3, chunk = idx & 7;
      const v4u o = *(const v4u*)(sm + ch * 32 + ((chunk ^ (ch >> 3)) << 2));
      ST128(o, WS_KVR + ((cb0 + ch) * S + (1984 - t0) + 8 * chunk) * 2);
    }
    if (mem < 24 && tid < 64) {
      const float a = (sf[tid] + sf[64 + tid]) + (sf[128 + tid] + sf[192 + tid]);
      ST32(__builtin_bit_cast(u32, a), WS_MODP + (kq * 3072 + mq * 512 + cb0 + tid) * 4);
    }
#pragma unroll
    for (int i = 0; i < 2; ++i) {
      float s = 0.f;
#pragma unroll
      for (int q = 0; q < 4; ++q) s += bf_lo(sr[i][q]) + bf_hi(sr[i][q]);
      const float m = wave_sum(s) * (1.f / D);
      float sq = 0.f;
#pragma unroll
      for (int q = 0; q < 4; ++q) {
        const float a = bf_lo(sr[i][q]) - m, e = bf_hi(sr[i][q]) - m;
        sq += a * a + e * e;
      }
      const float var = wave_sum(sq) * (1.f / D);
      st_pay[i] = pack_bf2(m, sqrtf(rbf(rbf(var) + 1e-5f)));
    }
#pragma unroll
    for (int i = 0; i < 2; ++i) *(v4u*)(smem + L_H + i * C_HCH + 64 + tid * 16) = hv[i];
  }
  const u32 tag = __builtin_amdgcn_readfirstlane(epoch_v) + 1u;
  STAMP(2);
  DRAIN();
  BAR();
  if (tid == 0) ST32(tag, WS_FLAG0 + (grp * 32 + mem) * 128);
  if (lane == 0) ST128((v4u{st_pay[0], tag, st_pay[1], tag}), WS_ST1 + (8 * b + 2 * w) * 8);
  STAMP(3);

  const int rt = 4 * grp + (mem >> 3), ct2 = mem & 7;
  const int ks0 = 11 * w;
  v2u w2r[11][8];
  const u16* w2p = w2 + (size_t)(32 * ks0 + 8 * g) * D + 64 * ct2 + 4 * c;
#define W2_LOAD(i)                                                                        \
  {                                                                                       \
    const int ii = ((i) == 10 && w == 3) ? 9 : (i);                                       \
    _Pragma("unroll") for (int jj = 0; jj < 8; ++jj) w2r[i][jj] = *(const v2u*)(w2p + (size_t)(32 * ii + jj) * D); \
  }
#pragma unroll
  for (int i = 0; i < W2_A; ++i) W2_LOAD(i);
  __builtin_amdgcn_sched_barrier(0);

  if (w == 0) {
    for (u32 sp = 0; sp < SPIN_CAP; ++sp) {
      CFENCE();
      const u32 f = LD32(WS_FLAG0 + (grp * 32 + (lane & 31)) * 128);
      if (__all(f == tag)) break;
    }
  }
  BAR();
  STAMP(4);

  const int rp = 2 * grp + (mem >> 4), ct1 = mem & 15;
  const int f0 = ct1 <= 12 ? 88 * ct1 : 1056 + 80 * (ct1 - 12);
  const int noct = ct1 < 12 ? 11 : 10;
  {
    v4u uv[2];
    u32 un[2];
#pragma unroll
    for (int i = 0; i < 2; ++i) {
      uv[i] = LD128(WS_KVR + ((c0 + i) * S + tid * 8) * 2);
      un[i] = LD32(WS_KVR + ((c0 + i) * S + tid * 8 + 8) * 2);
    }
    float mp[4];
    {
      const int l12 = lane < 12 ? lane : 0;
#pragma unroll
      for (int i = 0; i < 4; ++i) mp[i] = __builtin_bit_cast(float, LD32(WS_MODP + (i * 3072 + (l12 >> 1) * 512 + c0 + (l12 & 1)) * 4));
    }
    const int tb = 512 * w + 64 * g + 2 * c;
    v2u xv[4];
    v4u stv[4];
#pragma unroll
    for (int i = 0; i < 4; ++i) {
      const int t = tb + 256 * (i >> 1) + 32 * (i & 1);
      xv[i] = LD64(WS_XP + (pair * S + t) * 4);
      stv[i] = LD128(WS_ST1 + t * 8);
    }
    const u32 dd = *(const u32*)(ssmD + c0);
    __builtin_amdgcn_sched_barrier(0);
#pragma unroll
    for (int i = 0; i < 2; ++i) {
      *(v4u*)(smem + L_U + i * C_UCH + C_UCOPY + tid * 16) = uv[i];
      const u32 nx = tid < 255 ? un[i] : 0u;
      v4u s;
      s[0] = __builtin_amdgcn_alignbit(uv[i][1], uv[i][0], 16);
      s[1] = __builtin_amdgcn_alignbit(uv[i][2], uv[i][1], 16);
      s[2] = __builtin_amdgcn_alignbit(uv[i][3], uv[i][2], 16);
      s[3] = __builtin_amdgcn_alignbit(nx, uv[i][3], 16);
      *(v4u*)(smem + L_U + i * C_UCH + tid * 16) = s;
    }
    const float md = rbf((mp[0] + mp[1]) + (mp[2] + mp[3]));
    if (w == 0) {
      const float sbv = __shfl(md, 6 + (lane & 1)), cbv = __shfl(md, 8 + (lane & 1)), gbv = __shfl(md, 10 + (lane & 1));
      if (lane < 2) ST128((v4u{pack_bf2(1.f + cbv, sbv), tag, __builtin_bit_cast(u32, gbv), tag}), WS_FIN + (c0 + lane) * 16);
    }
    const float sa0 = rdlane(md, 0), sa1 = rdlane(md, 1), ga0 = rdlane(md, 4), ga1 = rdlane(md, 5);
    const float ca0 = rbf(1.f + rdlane(md, 2)), ca1 = rbf(1.f + rdlane(md, 3));
#define ST_OK() (stv[0][1] == tag && stv[0][3] == tag && stv[1][1] == tag && stv[1][3] == tag && stv[2][1] == tag && stv[2][3] == tag && stv[3][1] == tag && stv[3][3] == tag)
    if (!__all(ST_OK())) {
#pragma unroll 1
      for (u32 sp = 0; sp < SPIN_CAP; ++sp) {
        CFENCE();
#pragma unroll
        for (int i = 0; i < 4; ++i) stv[i] = LD128(WS_ST1 + (tb + 256 * (i >> 1) + 32 * (i & 1)) * 8);
        if (__all(ST_OK())) break;
      }
    }
#pragma unroll
    for (int i = 0; i < 4; ++i) asm volatile("" : "+v"(xv[i]));
    BAR();
    STAMP(5);
    const int ch = w >> 1, hf = w & 1;
    const int e = c & 1, y = c >> 1;
    const u32* hb = (const u32*)(smem + L_H + ch * C_HCH) + (c + 4 * g + 1 + 64 * hf);
    const unsigned char* ub = smem + L_U + ch * C_UCH + e * C_UCOPY + 2 * (2016 - 1792 - 32 * y + 8 * g) + 256 * hf;
    const unsigned char* w1src[6];
#pragma unroll
    for (int n = 0; n < 6; ++n) {
      const int i = 64 * n + lane, row = i / 12, slot = i % 12;
      w1src[n] = (const unsigned char*)(w1 + (size_t)(128 * w + row) * F + f0 + 8 * (slot < noct ? slot : 0));
    }
    const u32 lbase = (u32)(uintptr_t)(LAS unsigned char*)smem + L_W1 + w * 24576;
    f32x4 acc[8];
#pragma unroll
    for (int n = 0; n < 8; ++n) acc[n] = f32x4{0.f, 0.f, 0.f, 0.f};
    v4u hfr[2][8], ufr[2][8];
#define CONV_LD(buf, rr, s)                                                          \
  {                                                                                  \
    ufr[buf][s] = *(const v4u*)(ub + 64 * (rr) + 512 * (7 - (s)));                   \
    _Pragma("unroll") for (int jj = 0; jj < 4; ++jj) hfr[buf][s][jj] = hb[16 * (rr) + 128 * (s) + jj]; \
  }
#pragma unroll
    for (int s = 0; s < 8; ++s) CONV_LD(0, 0, s);
    __builtin_amdgcn_sched_barrier(0);
#pragma unroll
    for (int rr = 0; rr < 4; ++rr) {
#pragma unroll
      for (int n = 0; n < 8; ++n) {
        if (rr < 3) CONV_LD((rr + 1) & 1, rr + 1, n);
        if (n >= 2) glds16(w1src[n - 2] + (size_t)rr * 32 * F * 2, lbase + rr * 6144 + (n - 2) * 1024);
#pragma unroll
        for (int s = 0; s <= n; ++s) acc[n] = mfma16(ufr[rr & 1][n - s], hfr[rr & 1][s], acc[n]);
        __builtin_amdgcn_sched_barrier(0);
      }
    }
    STAMP(6);
    f32x4* red = (f32x4*)(smem + L_RED);
#pragma unroll
    for (int n = 0; n < 8; ++n) red[(w * 8 + n) * 64 + lane] = acc[n];
    BAR();
    const float d0 = bf_lo(dd), d1 = bf_hi(dd);
    const unsigned char* u1a = smem + L_U + C_UCOPY;
#pragma unroll
    for (int i = 0; i < 2; ++i) {
      const int n = 2 * w + i;
      const f32x4 y0 = red[(0 * 8 + n) * 64 + lane] + red[(1 * 8 + n) * 64 + lane];
      const f32x4 y1 = red[(2 * 8 + n) * 64 + lane] + red[(3 * 8 + n) * 64 + lane];
#pragma unroll
      for (int h = 0; h < 2; ++h) {
        const int t = tb + 256 * i + 32 * h;
        const u32 kva = *(const u32*)(u1a + (2046 - t) * 2), kvb = *(const u32*)(u1a + C_UCH + (2046 - t) * 2);
        v2u o;
#pragma unroll
        for (int r = 0; r < 2; ++r) {
          const float kv0 = r ? bf_lo(kva) : bf_hi(kva), kv1 = r ? bf_lo(kvb) : bf_hi(kvb);
          const u32 xw = xv[2 * i + h][r], sw = stv[2 * i + h][2 * r];
          const float x0 = bf_lo(xw), x1 = bf_hi(xw);
          const float mean = bf_lo(sw), rstd = __builtin_amdgcn_rcpf(bf_hi(sw));
          const float z0 = y0[2 * h + r] + kv0 * d0, z1 = y1[2 * h + r] + kv1 * d1;
          const float h0 = (x0 - mean) * rstd * ca0 + sa0;
          const float h1 = (x1 - mean) * rstd * ca1 + sa1;
          o[r] = pack_bf2(x0 + ga0 * (h0 * z0), x1 + ga1 * (h1 * z1));
        }
        ST64(o, WS_X1P + (pair * S + t) * 4);
      }
    }
  }
  STAMP(7);
  DRAIN();
  BAR();
  if (tid < 32) __hip_atomic_fetch_add((GAS u32*)(ws + WS_CNT1 + (tid * 8 + grp) * 256), 1u, __ATOMIC_RELAXED, __HIP_MEMORY_SCOPE_AGENT);
  STAMP(8);

  {
    v4u fin[2];
#pragma unroll
    for (int i = 0; i < 2; ++i) fin[i] = LD128(WS_FIN + (128 * w + 2 * lane + i) * 16);
    const u16 b1v = b1[f0 + ((tid >= 96 && tid - 96 < 8 * noct) ? tid - 96 : 0)];
#pragma unroll
    for (int i = W2_A; i < W2_A + W2_C; ++i) W2_LOAD(i);
    __builtin_amdgcn_sched_barrier(0);
    if (!__all(fin[0][1] == tag && fin[0][3] == tag && fin[1][1] == tag && fin[1][3] == tag)) {
#pragma unroll 1
      for (u32 sp = 0; sp < SPIN_CAP; ++sp) {
        CFENCE();
#pragma unroll
        for (int i = 0; i < 2; ++i) fin[i] = LD128(WS_FIN + (128 * w + 2 * lane + i) * 16);
        if (__all(fin[0][1] == tag && fin[0][3] == tag && fin[1][1] == tag && fin[1][3] == tag)) break;
      }
    }
    *(u32*)(smem + L_SA + (128 * w + 2 * lane) * 2) = __builtin_amdgcn_perm(fin[1][0], fin[0][0], 0x05040100u);
    *(u32*)(smem + L_SB + (128 * w + 2 * lane) * 2) = __builtin_amdgcn_perm(fin[1][0], fin[0][0], 0x07060302u);
    *(u32*)(smem + L_GB + (128 * w + 2 * lane) * 2) = __builtin_amdgcn_perm(fin[1][2], fin[0][2], 0x07060302u);
    f32x4 a3[6];
#pragma unroll
    for (int j = 0; j < 6; ++j) a3[j] = f32x4{0.f, 0.f, 0.f, 0.f};
    const bool dact = g == (c >> 2);
    const unsigned char* trb = smem + L_W1 + w * 24576 + (8 * g + ((lane >> 2) & 3)) * 192 + (lane & 3) * 16;
    const bool ones = (c >> 2) == 3;
#pragma unroll
    for (int q = 0; q < 4; ++q) {
      const int kb = 128 * w + 32 * q + 8 * g;
      const u32 sv1 = *(const u16*)(smem + L_SA + (kb + (c & 3)) * 2), sv2 = *(const u16*)(smem + L_SA + (kb + 4 + (c & 3)) * 2);
      const u32 val1 = dact ? (sv1 << (16 * (c & 1))) : 0u, val2 = dact ? (sv2 << (16 * (c & 1))) : 0u;
      const v4u A1 = {(c & 2) ? 0u : val1, (c & 2) ? val1 : 0u, 0u, 0u};
      const v4u A2 = {0u, 0u, (c & 2) ? 0u : val2, (c & 2) ? val2 : 0u};
      v4u A3 = *(const v4u*)(smem + (c == 1 ? L_SB : L_SA) + kb * 2);
      if (c >= 2) A3 = v4u{0u, 0u, 0u, 0u};
      v4u raw[6];
#pragma unroll
      for (int j = 0; j < 6; ++j) {
        const unsigned char* p = trb + q * 6144 + (j >> 1) * 64 + (j & 1) * 8;
        const v2u lo = __builtin_bit_cast(v2u, __builtin_amdgcn_ds_read_tr16_b64_v4i16((LAS v4s*)p));
        const v2u hi = __builtin_bit_cast(v2u, __builtin_amdgcn_ds_read_tr16_b64_v4i16((LAS v4s*)(p + 4 * 192)));
        raw[j] = v4u{lo[0], lo[1], hi[0], hi[1]};
      }
      asm volatile("s_waitcnt lgkmcnt(0)" ::: "memory");
#pragma unroll
      for (int j = 0; j < 6; ++j) {
        const f32x4 z = {0.f, 0.f, 0.f, 0.f};
        const f32x4 p1 = mfma16(A1, raw[j], z), p2 = mfma16(A2, raw[j], z);
        a3[j] = mfma16(A3, raw[j], a3[j]);
        v4u o = {pack_bf2(p1[0], p1[1]), pack_bf2(p1[2], p1[3]), pack_bf2(p2[0], p2[1]), pack_bf2(p2[2], p2[3])};
        if (j >= 4 && ones) o = v4u{0x3f803f80u, 0x3f803f80u, 0x3f803f80u, 0x3f803f80u};
        *(v4u*)(smem + L_W1 + w * 24576 + q * 6144 + j * 1024 + lane * 16) = o;
      }
    }
    float* csp = (float*)(smem + L_CSP);
    if (g == 0) {
#pragma unroll
      for (int j = 0; j < 6; ++j) {
        const int fc = 32 * (j >> 1) + 8 * (c >> 2) + 4 * (j & 1) + (c & 3);
        csp[w * 192 + fc] = a3[j][0];
        csp[w * 192 + 96 + fc] = a3[j][1];
      }
    }
    BAR();
    if (tid < 192) {
      float cv = (csp[tid] + csp[192 + tid]) + (csp[384 + tid] + csp[576 + tid]);
      if (tid >= 96) cv += bf2f(b1v);
      ((float*)(smem + L_CS))[tid] = cv;
    }
  }
  STAMP(9);
  if (w == 0) {
    for (u32 sp = 0; sp < SPIN_CAP; ++sp) {
      CFENCE();
      const u32 f = LD32(WS_CNT1 + ((b & 31) * 8 + (lane & 7)) * 256);
      if (__all(f == 32u)) break;
    }
    if (b == 0 && lane == 0) ST32(tag, WS_EPOCH);
  }
  BAR();
  STAMP(10);

  {
    const int t0 = 128 * rp + 32 * w;
    v2u xl[16][4];
    constexpr int PF1 = 5;
#define X_LD(ks) \
  { _Pragma("unroll") for (int i = 0; i < 4; ++i) xl[ks][i] = LD64(WS_X1P + ((16 * (ks) + 4 * g + i) * S + t0 + 2 * c) * 4); }
#pragma unroll
    for (int ks = 0; ks < PF1; ++ks) X_LD(ks);
    f32x4 acc[6][2], sq[2];
#pragma unroll
    for (int tt = 0; tt < 2; ++tt) {
      sq[tt] = f32x4{0.f, 0.f, 0.f, 0.f};
#pragma unroll
      for (int j = 0; j < 6; ++j) acc[j][tt] = f32x4{0.f, 0.f, 0.f, 0.f};
    }
    const v4u* wl = (const v4u*)(smem + L_W1) + lane;
    v4u wf[2][6];
#pragma unroll
    for (int j = 0; j < 6; ++j) wf[0][j] = wl[j * 64];
    __builtin_amdgcn_sched_barrier(0);
#pragma unroll
    for (int ks = 0; ks < 16; ++ks) {
      if (ks + PF1 < 16) X_LD(ks + PF1);
      if (ks + 1 < 16) {
#pragma unroll
        for (int j = 0; j < 6; ++j) wf[(ks + 1) & 1][j] = wl[((ks + 1) * 6 + j) * 64];
      }
#pragma unroll
      for (int tt = 0; tt < 2; ++tt) {
        const v4u xf = {xl[ks][0][tt], xl[ks][1][tt], xl[ks][2][tt], xl[ks][3][tt]};
#pragma unroll
        for (int j = 0; j < 6; ++j) acc[j][tt] = mfma16(wf[ks & 1][j], xf, acc[j][tt]);
        sq[tt] = mfma16(xf, xf, sq[tt]);
      }
#pragma unroll
      for (int i = 0; i < 4; ++i) {
        __builtin_amdgcn_sched_group_barrier(0x2, 4, 0);
        __builtin_amdgcn_sched_group_barrier(0x8, 1, 0);
        __builtin_amdgcn_sched_group_barrier(0x20, 1, 0);
        __builtin_amdgcn_sched_group_barrier(0x8, 1, 0);
        __builtin_amdgcn_sched_group_barrier(0x100, 2, 0);
        __builtin_amdgcn_sched_group_barrier(0x8, 1, 0);
      }
      __builtin_amdgcn_sched_barrier(0);
    }
    STAMP(11);
    const float* cs = (const float*)(smem + L_CS);
#pragma unroll
    for (int tt = 0; tt < 2; ++tt) {
      const float sum = __shfl(acc[4][tt][0], 48 + c);
      const int r = c & 3;
      const float dsel = r == 0 ? sq[tt][0] : r == 1 ? sq[tt][1] : r == 2 ? sq[tt][2] : sq[tt][3];
      const float ssq = __shfl(dsel, c + 16 * (c >> 2));
      const float m = sum * (1.f / D);
      const float var = fmaxf(ssq * (1.f / D) - m * m, 0.f);
      const float rsd = __builtin_amdgcn_rcpf(rbf(__builtin_amdgcn_sqrtf(rbf(rbf(var) + 1e-5f))));
      const float mr = -rbf(m) * rsd;
#pragma unroll
      for (int jh = 0; jh < 3; ++jh) {
        const int slot = 4 * jh + g;
        v4u o;
#pragma unroll
        for (int jl = 0; jl < 2; ++jl) {
          const f32x4 u1 = *(const f32x4*)(cs + 8 * slot + 4 * jl), s1 = *(const f32x4*)(cs + 96 + 8 * slot + 4 * jl);
          float hq[4];
#pragma unroll
          for (int q = 0; q < 4; ++q) {
            float a = acc[2 * jh + jl][tt][q] * rsd + (mr * u1[q] + s1[q]);
            a = fmaxf(a, 0.f);
            hq[q] = a * a;
          }
          o[2 * jl] = pack_bf2(hq[0], hq[1]);
          o[2 * jl + 1] = pack_bf2(hq[2], hq[3]);
        }
        if (slot < noct) ST128(o, WS_HO + (((f0 >> 3) + slot) * S + t0 + 16 * tt + c) * 16);
      }
    }
  }
  DRAIN();
  BAR();
  if (tid == 0) ST32(tag, WS_FLAG2 + (rp * 16 + ct1) * 128);
  STAMP(12);

  const int te = 64 * rt + 32 * (w >> 1) + 2 * c + (w & 1);
  u32 x1v[8];
  v4u b2v[2];
#pragma unroll
  for (int i = W2_A + W2_C; i < 11; ++i) W2_LOAD(i);
#pragma unroll
  for (int i = 0; i < 8; ++i) x1v[i] = LD32(WS_X1P + ((32 * ct2 + 8 * g + i) * S + te) * 4);
#pragma unroll
  for (int i = 0; i < 2; ++i) b2v[i] = *(const v4u*)(b2 + 64 * ct2 + 16 * g + 8 * i);
  __builtin_amdgcn_sched_barrier(0);
  v4u wf2[11][4];
#pragma unroll
  for (int i = 0; i < 11; ++i)
#pragma unroll
    for (int j = 0; j < 4; ++j)
#pragma unroll
      for (int p = 0; p < 4; ++p)
        wf2[i][j][p] = __builtin_amdgcn_perm(w2r[i][2 * p + 1][j >> 1], w2r[i][2 * p][j >> 1], (j & 1) ? 0x07060302u : 0x05040100u);
  __builtin_amdgcn_sched_barrier(0);
  if (w == 0) {
    for (u32 sp = 0; sp < SPIN_CAP; ++sp) {
      CFENCE();
      const u32 f = LD32(WS_FLAG2 + ((rt >> 1) * 16 + c) * 128);
      if (__all(f == tag)) break;
    }
  }
  BAR();
  STAMP(13);

  {
    v4u hf[11][4];
    constexpr int PF2 = 4;
#define H_LD(i)                                                                                                        \
  {                                                                                                                    \
    const int ii = ((i) == 10 && w == 3) ? 9 : (i);                                                                    \
    _Pragma("unroll") for (int q = 0; q < 4; ++q) hf[i][q] = LD128(WS_HO + ((4 * (ks0 + ii) + g) * S + 64 * rt + 16 * q + c) * 16); \
  }
#pragma unroll
    for (int i = 0; i < PF2; ++i) H_LD(i);
    f32x4 acc[4][4];
#pragma unroll
    for (int j = 0; j < 4; ++j)
#pragma unroll
      for (int q = 0; q < 4; ++q) acc[j][q] = f32x4{0.f, 0.f, 0.f, 0.f};
    __builtin_amdgcn_sched_barrier(0);
#pragma unroll
    for (int i = 0; i < 11; ++i) {
      if (i + PF2 < 11) H_LD(i + PF2);
      if (i < 10 || w < 3) {
#pragma unroll
        for (int q = 0; q < 4; ++q)
#pragma unroll
          for (int j = 0; j < 4; ++j) acc[j][q] = mfma16(wf2[i][j], hf[i][q], acc[j][q]);
      }
#pragma unroll
      for (int q = 0; q < 4; ++q) {
        __builtin_amdgcn_sched_group_barrier(0x8, 1, 0);
        __builtin_amdgcn_sched_group_barrier(0x2, 2, 0);
        __builtin_amdgcn_sched_group_barrier(0x20, 1, 0);
        __builtin_amdgcn_sched_group_barrier(0x8, 3, 0);
      }
      __builtin_amdgcn_sched_barrier(0);
    }
    STAMP(14);
    f32x4* red = (f32x4*)(smem + L_W1);
#pragma unroll
    for (int j = 0; j < 4; ++j)
#pragma unroll
      for (int q = 0; q < 4; ++q) red[((w * 4 + q) * 4 + j) * 64 + lane] = acc[j][q];
    BAR();
    f32x4 a[4];
#pragma unroll
    for (int j = 0; j < 4; ++j)
      a[j] = (red[((0 * 4 + w) * 4 + j) * 64 + lane] + red[((1 * 4 + w) * 4 + j) * 64 + lane]) +
             (red[((2 * 4 + w) * 4 + j) * 64 + lane] + red[((3 * 4 + w) * 4 + j) * 64 + lane]);
#pragma unroll
    for (int h = 0; h < 2; ++h) {
      const v4u gbv = *(const v4u*)(smem + L_GB + (64 * ct2 + 16 * g + 8 * h) * 2);
      v4u o;
#pragma unroll
      for (int p = 0; p < 4; ++p) {
        const int reg = 2 * h + (p >> 1), j = 2 * (p & 1);
        const u32 xw = x1v[4 * h + p], bw = b2v[h][p], gw = gbv[p];
        o[p] = pack_bf2(bf_lo(xw) + bf_lo(gw) * (a[j][reg] + bf_lo(bw)), bf_hi(xw) + bf_hi(gw) * (a[j + 1][reg] + bf_hi(bw)));
      }
      *(v4u*)(out + (size_t)te * D + 64 * ct2 + 16 * g + 8 * h) = o;
    }
  }
}

extern "C" void kernel_launch(void* const* d_in, const int* in_sizes, int n_in, void* d_out, int out_size, void* d_ws, size_t ws_size,
                              hipStream_t stream) {
  (void)hipFuncSetAttribute((const void*)mega, hipFuncAttributeMaxDynamicSharedMemorySize, L_END);
  mega<<<256, 256, L_END, stream>>>((const u16*)d_in[0], (const u16*)d_in[1], (const u16*)d_in[2], (const u16*)d_in[3], (const u16*)d_in[4],
                                    (const u16*)d_in[5], (const u16*)d_in[7], (const u16*)d_in[8], (const u16*)d_in[9], (const u16*)d_in[10],
                                    (const u16*)d_in[11], (const u16*)d_in[12], (unsigned char*)d_ws, (u16*)d_out);
}
```

```cpp
#include <hip/hip_runtime.h>
#include <hip/hip_bf16.h>
#include <stdint.h>

typedef unsigned int u32;
typedef unsigned short u16;
typedef u32 v4u __attribute__((ext_vector_type(4)));
typedef u32 v2u __attribute__((ext_vector_type(2)));
typedef float f32x4 __attribute__((ext_vector_type(4)));
typedef float f32x2 __attribute__((ext_vector_type(2)));
typedef __bf16 bf16x8 __attribute__((ext_vector_type(8)));
typedef __bf16 bf16x2 __attribute__((ext_vector_type(2)));
typedef short v4s __attribute__((ext_vector_type(4)));
#define LAS __attribute__((address_space(3)))
#define GAS __attribute__((address_space(1)))

constexpr int S = 2048, D = 512, F = 1376;

constexpr int WS_EPOCH = 0;
constexpr int WS_FLAG0 = 4096;
constexpr int WS_FLAG2 = WS_FLAG0 + 256 * 128;
constexpr int WS_CNT1 = WS_FLAG2 + 256 * 128;
constexpr int WS_ST1 = WS_CNT1 + 256 * 256;
constexpr int WS_FIN = WS_ST1 + S * 8;
constexpr int WS_MODP = WS_FIN + 256 * 128;
constexpr int KV_STR = 4096 + 128;
constexpr int XP_STR = S * 4 + 128;
constexpr int HO_STR = S * 16 + 256;
constexpr int WS_KVR = 1 << 20;
constexpr int WS_X1P = WS_KVR + 1025 * KV_STR + 128;
constexpr int WS_HO = WS_X1P + 256 * XP_STR;
constexpr int WS_END = WS_HO + (F / 8) * HO_STR;
static_assert(WS_X1P % 128 == 0 && WS_MODP + 16 * 3072 * 8 <= WS_KVR, "ctrl region");

constexpr int L_W1 = 0;
constexpr int C_HCH = 64 + 4096;
constexpr int C_UCOPY = 4544 + 32;
constexpr int C_UCH = 2 * C_UCOPY;
constexpr int L_H = 98304, L_U = L_H + 2 * C_HCH, L_RED = L_U + 2 * C_UCH, L_MISC = L_RED + 32768;
constexpr int L_SA = L_MISC, L_SB = L_SA + 1024, L_GB = L_SB + 1024, L_CS = L_GB + 1024, L_CNT = L_CS + 768, L_END = L_CNT + 64;
constexpr int L_CSP = L_H;
constexpr int L_XCH = L_H;
static_assert(L_U % 16 == 0 && L_RED % 16 == 0 && L_END <= 163840 && L_XCH + 57344 <= L_MISC, "lds");

constexpr u32 SPIN_CAP = 1u << 16;
#define XNOW2 0
#define XMODQ 6
#define XNOSR 0
#define MSLEEP0 8
#define MSLEEP 4
#define CODE_BYTES 20480
#define DSLEEP 2
#define HLATE 0
#define DIAG1 0
#define FSLEEP 8
#define BNT ""
#define ROT2 1
#define PF1 10
#define NW2G 4
#define MAP1 0
#define MAP2 0
#define NW2C 6
#define PSLEEP 10
#define HDELAY 0

#define L_TOT L_END
#define STAMP(i)

__device__ __forceinline__ float bf_lo(u32 v) { return __builtin_bit_cast(float, v << 16); }
__device__ __forceinline__ float bf_hi(u32 v) { return __builtin_bit_cast(float, v & 0xffff0000u); }
__device__ __forceinline__ f32x2 bf_pair(u32 v) { return f32x2{bf_lo(v), bf_hi(v)}; }
__device__ __forceinline__ float bf2f(u16 v) { return __builtin_bit_cast(float, (u32)v << 16); }
__device__ __forceinline__ u32 pack2(f32x2 v) { return __builtin_bit_cast(u32, __builtin_convertvector(v, bf16x2)); }
__device__ __forceinline__ u32 pack_bf2(float lo, float hi) { return pack2(f32x2{lo, hi}); }
__device__ __forceinline__ float rbf(float x) { return bf_lo(pack_bf2(x, 0.f)); }
__device__ __forceinline__ f32x2 fma2(f32x2 a, f32x2 b, f32x2 c) { return __builtin_elementwise_fma(a, b, c); }
__device__ __forceinline__ f32x2 splat(float a) { return f32x2{a, a}; }
__device__ __forceinline__ float dot2(u32 a, u32 b, float c) {
  return __builtin_amdgcn_fdot2_f32_bf16(__builtin_bit_cast(bf16x2, a), __builtin_bit_cast(bf16x2, b), c, false);
}
__device__ __forceinline__ u32 perm_lo(u32 hi, u32 lo) { return __builtin_amdgcn_perm(hi, lo, 0x05040100u); }
__device__ __forceinline__ u32 perm_hi(u32 hi, u32 lo) { return __builtin_amdgcn_perm(hi, lo, 0x07060302u); }
__device__ __forceinline__ f32x4 mfma16(v4u a, v4u b, f32x4 c) {
  return __builtin_amdgcn_mfma_f32_16x16x32_bf16(__builtin_bit_cast(bf16x8, a), __builtin_bit_cast(bf16x8, b), c, 0, 0, 0);
}
typedef float f32x16 __attribute__((ext_vector_type(16)));
__device__ __forceinline__ f32x16 mfma32(v4u a, v4u b, f32x16 c) {
  return __builtin_amdgcn_mfma_f32_32x32x16_bf16(__builtin_bit_cast(bf16x8, a), __builtin_bit_cast(bf16x8, b), c, 0, 0, 0);
}
template <int CTRL> __device__ __forceinline__ float dpp_add(float v) {
  return v + __builtin_bit_cast(float, __builtin_amdgcn_update_dpp(0, __builtin_bit_cast(int, v), CTRL, 0xf, 0xf, true));
}
__device__ __forceinline__ float fold32(float a, float b) {
  auto r = __builtin_amdgcn_permlane32_swap(__builtin_bit_cast(u32, a), __builtin_bit_cast(u32, b), false, false);
  return __builtin_bit_cast(float, (u32)r[0]) + __builtin_bit_cast(float, (u32)r[1]);
}
__device__ __forceinline__ float fold16(float a, float b) {
  auto r = __builtin_amdgcn_permlane16_swap(__builtin_bit_cast(u32, a), __builtin_bit_cast(u32, b), false, false);
  return __builtin_bit_cast(float, (u32)r[0]) + __builtin_bit_cast(float, (u32)r[1]);
}
__device__ __forceinline__ float row_sum(float v) {
  v = dpp_add<0xB1>(v);
  v = dpp_add<0x4E>(v);
  v = dpp_add<0x141>(v);
  return dpp_add<0x140>(v);
}
__device__ __forceinline__ float rdlane(float v, int l) { return __builtin_bit_cast(float, __builtin_amdgcn_readlane(__builtin_bit_cast(int, v), l)); }
__device__ __forceinline__ v4u ldnt(const u16* p) { return __builtin_nontemporal_load((const v4u*)p); }
__device__ __forceinline__ void dma_w1x6(v4u rsrc, const u32 (&vo)[6], u32 soff, u32 lds_dst) {
  u32 keep, ld;
  asm volatile(
      "s_mov_b32 %0, m0\n\ts_mov_b32 m0, %10\n\ts_add_u32 %1, %10, 0x400\n\t"
      "buffer_load_dwordx4 %2, %8, %9 offen lds\n\ts_mov_b32 m0, %1\n\ts_add_u32 %1, %1, 0x400\n\t"
      "buffer_load_dwordx4 %3, %8, %9 offen lds\n\ts_mov_b32 m0, %1\n\ts_add_u32 %1, %1, 0x400\n\t"
      "buffer_load_dwordx4 %4, %8, %9 offen lds\n\ts_mov_b32 m0, %1\n\ts_add_u32 %1, %1, 0x400\n\t"
      "buffer_load_dwordx4 %5, %8, %9 offen lds\n\ts_mov_b32 m0, %1\n\ts_add_u32 %1, %1, 0x400\n\t"
      "buffer_load_dwordx4 %6, %8, %9 offen lds\n\ts_mov_b32 m0, %1\n\ts_nop 0\n\t"
      "buffer_load_dwordx4 %7, %8, %9 offen lds\n\t"
      "s_mov_b32 m0, %0"
      : "=&s"(keep), "=&s"(ld)
      : "v"(vo[0]), "v"(vo[1]), "v"(vo[2]), "v"(vo[3]), "v"(vo[4]), "v"(vo[5]), "s"(rsrc), "s"(soff), "s"(lds_dst)
      : "scc");
}
#define SC1 16
#define NT 2
#define LD128(off, so) __builtin_amdgcn_raw_buffer_load_b128(rs, (off), (so), SC1)
#define LD64(off, so) __builtin_amdgcn_raw_buffer_load_b64(rs, (off), (so), SC1)
#define LD32(off, so) __builtin_amdgcn_raw_buffer_load_b32(rs, (off), (so), SC1)
#define ST128(v, off, so) __builtin_amdgcn_raw_buffer_store_b128((v), rs, (off) + (so), 0, SC1)
#define ST64(v, off, so) __builtin_amdgcn_raw_buffer_store_b64((v), rs, (off), (so), SC1)
#define ST32(v, off, so) __builtin_amdgcn_raw_buffer_store_b32((v), rs, (off), (so), SC1)
#define DRAIN() asm volatile("s_waitcnt vmcnt(0)" ::: "memory")
#define CFENCE() asm volatile("" ::: "memory")
#define SB() __builtin_amdgcn_sched_barrier(0)
#define WAITV4(n, a, b, c, d) asm volatile("s_waitcnt vmcnt(" #n ")" : "+v"(a), "+v"(b), "+v"(c), "+v"(d)::"memory")
#define BAR() asm volatile("s_waitcnt lgkmcnt(0)\n\ts_barrier" ::: "memory")
#define BAR_M() asm volatile("s_waitcnt lgkmcnt(0)\n\ts_barrier ; main" ::: "memory")
#define BAR_H() asm volatile("s_waitcnt lgkmcnt(0)\n\ts_barrier ; helper" ::: "memory")

typedef u32 v8u __attribute__((ext_vector_type(8)));
typedef u32 v16u __attribute__((ext_vector_type(16)));
struct Params {
  const void* role[3][5];
  const void* pad;
  const void* rest[8];
};
__device__ __forceinline__ unsigned long long mk64(u32 lo, u32 hi) { return ((unsigned long long)hi << 32) | lo; }

__global__ void __launch_bounds__(512, 2) mega(Params prm) {
  extern __shared__ __attribute__((aligned(16))) unsigned char smem[];
  int b = blockIdx.x;
  const int tid = threadIdx.x, lane = tid & 63, t8 = tid & 255;
  const int w = __builtin_amdgcn_readfirstlane(tid >> 6);
  STAMP(0);
  typedef const __attribute__((address_space(4))) unsigned char* kptr;
  const kptr kp = (kptr)__builtin_amdgcn_kernarg_segment_ptr();
  const kptr kr_ = kp + (w < 4 ? 0 : (w >> 1) - 1) * 40;
  typedef u32 v8a __attribute__((ext_vector_type(8), aligned(8)));
  typedef u32 v16a __attribute__((ext_vector_type(16), aligned(16)));
  const v8u ka = *(const __attribute__((address_space(4))) v8a*)kr_;
  const v2u ke = *(const __attribute__((address_space(4))) v2u*)(kr_ + 32);
  const v16u kb = *(const __attribute__((address_space(4))) v16a*)(kp + 0x80);
  unsigned long long pc;
  asm volatile("s_getpc_b64 %0" : "=s"(pc));
  SB();
  const int wl = w & 3;
  const bool mainw = w < 4;
  const int grp = b & 7, mem = b >> 3;
  const int c = lane & 15, g = lane >> 4;
  const int pair = grp * 32 + mem, c0 = 2 * pair;
  const int rp = 2 * grp + (mem >> 4), ct1 = mem & 15, rot1 = ct1 & 7, kfirst = (mem >> 4) * 8;
  const int f0 = 80 * ct1 + 8 * (ct1 < 12 ? ct1 : 12);
  const int noct = ct1 < 12 ? 11 : 10;
  const int rt = 4 * grp + (mem >> 3), ct2 = mem & 7;
  const int ks0 = 5 * w + (w < 3 ? w : 3);
  const int co = lane & 7, tq = t8 >> 3;
  const int ta = mem * 64 + 2 * tq;
  const int cb = grp * 64 + 8 * co;
  LAS u32* lcnt = (LAS u32*)(smem + L_CNT);
  if (tid < 16) lcnt[tid] = 0u;
  BAR();
  const int mq = mem % 6, kq = mem < 24 ? mem / 6 : 0;

  v4u L[8], Le;
  {
    const bool ww = (w >> 1) == 2;
    int bM = (mem * 64 + 16 * w) * 1024 + grp * 128, bW = (kq * 128 + 64 * wl) * 6144 + mq * 1024 + grp * 128, bX = (8 * b + 4 * (w - 6)) * 1024;
    int bH = (c0 + w - 6) * 4096;
    asm volatile("" : "+s"(bM), "+s"(bW), "+s"(bX), "+s"(bH));
    bW = (mem < 24 && mq < XMODQ) ? bW : 0x70000000;
    if (XNOSR) bX = 0x70000000;
    const int rstr = mainw ? 2048 : ww ? 8 * 6144 : 128;
    const int step = ww ? 6144 : 1024;
    const int baseA = mainw ? bM : ww ? bW : bX;
    const int base2 = baseA + (mainw ? 0 : 2 * step);
    const int base4 = w >= 6 ? bH : baseA + (mainw ? 0 : 4 * 6144);
    const int va = (lane >> 3) * rstr + co * 16;
    int o[8];
    o[0] = va + baseA;
    o[1] = va + (baseA + step);
    o[2] = va + base2;
    o[3] = va + (base2 + step);
    o[4] = va + base4;
    o[5] = va + (base4 + step);
    int o6a = lane < 24 ? bM - 3072 + (lane >> 3) * 1024 + co * 16 : 0x70000000, o6b = va + (base4 + 2 * step);
    int o7a = (grp * 512 + lane * 16) | ((lane >> 5) << 30), o7b = va + (base4 + 3 * step);
    int oc = (kq * 128 + 64 * wl) * 2 + (lane >> 3) * 16, op = mem * 1024 + lane * 16;
    asm volatile("" : "+v"(o6a), "+v"(o6b), "+v"(o7a), "+v"(o7b), "+v"(oc), "+v"(op));
    o[6] = mainw ? o6a : o6b;
    o[7] = mainw ? o7a : o7b;
    int oe = ww ? oc : op;
    int nb = ww ? D * 3072 * 2 : S * D * 2, nbD = mainw ? D * 4 * 2 : nb, nbE = ww ? 1024 : w == 6 ? CODE_BYTES : 0;
#pragma unroll
    for (int i = 0; i < 8; ++i) asm volatile("" : "+v"(o[i]));
    asm volatile("" : "+v"(oe), "+s"(nb), "+s"(nbD), "+s"(nbE));
    const v4u rA = {ka[0], ka[1], (u32)nb, 0x00020000u}, rB = {ka[2], ka[3], (u32)nb, 0x00020000u};
    const v4u rC = {ka[4], ka[5], (u32)nb, 0x00020000u}, rD = {ka[6], ka[7], (u32)nbD, 0x00020000u};
    const v4u rF = {ke[0], ke[1], (u32)nb, 0x00020000u};
    const v4u rE = {ww ? kb[0] : ((u32)pc & ~127u), ww ? kb[1] : (u32)(pc >> 32), (u32)nbE, 0x00020000u};
    asm volatile(
        "buffer_load_dwordx4 %8, %17, %23, 0 offen\n\t"
        "buffer_load_dwordx4 %0, %9, %18, 0 offen" BNT "\n\tbuffer_load_dwordx4 %1, %10, %18, 0 offen" BNT "\n\t"
        "buffer_load_dwordx4 %2, %11, %19, 0 offen" BNT "\n\tbuffer_load_dwordx4 %3, %12, %19, 0 offen" BNT "\n\t"
        "buffer_load_dwordx4 %6, %15, %22, 0 offen" BNT "\n\tbuffer_load_dwordx4 %7, %16, %21, 0 offen" BNT "\n\t"
        "buffer_load_dwordx4 %4, %13, %20, 0 offen" BNT "\n\tbuffer_load_dwordx4 %5, %14, %20, 0 offen" BNT ""
        : "=&v"(L[0]), "=&v"(L[1]), "=&v"(L[2]), "=&v"(L[3]), "=&v"(L[4]), "=&v"(L[5]), "=&v"(L[6]), "=&v"(L[7]), "=&v"(Le)
        : "v"(o[0]), "v"(o[1]), "v"(o[2]), "v"(o[3]), "v"(o[4]), "v"(o[5]), "v"(o[6]), "v"(o[7]), "v"(oe), "s"(rA), "s"(rB), "s"(rC), "s"(rD),
          "s"(rF), "s"(rE));
  }
  GAS unsigned char* ws = (GAS unsigned char*)mk64(kb[2], kb[3]);
  const GAS u16* ssmD = (const GAS u16*)mk64(kb[4], kb[5]);
  const GAS u16* w1 = (const GAS u16*)mk64(kb[6], kb[7]);
  const GAS u16* b1 = (const GAS u16*)mk64(kb[8], kb[9]);
  const GAS u16* w2 = (const GAS u16*)mk64(kb[10], kb[11]);
  const GAS u16* b2 = (const GAS u16*)mk64(kb[12], kb[13]);
  GAS u16* out = (GAS u16*)mk64(kb[14], kb[15]);
  const __amdgpu_buffer_rsrc_t rs = __builtin_amdgcn_make_buffer_rsrc((void*)ws, 0, WS_END, 0x00020000);
  u32 tag = *(const __attribute__((address_space(4))) u32*)(unsigned long long)ws + 1u;
  u32 dd = *(const __attribute__((address_space(4))) u32*)(unsigned long long)(ssmD + c0);
  SB();
  STAMP(29);

  v2u w2r[6][8];
  const int rot2 = ROT2 ? (rt + ct2) % 5 : 0;
#define KS2(i) ((i) < 5 ? ((i) + rot2 >= 5 ? (i) + rot2 - 5 : (i) + rot2) : 5)
  const __amdgpu_buffer_rsrc_t rw2 = __builtin_amdgcn_make_buffer_rsrc((void*)w2, 0, F * D * 2, 0x00020000);
  const int w2v = (8 * g * D + 4 * c) * 2, w2s = (32 * ks0 * D + 64 * ct2) * 2;
#define W2_LOAD(i)                                                                                                   \
  if ((i) < 5 || w < 3) {                                                                                            \
    _Pragma("unroll") for (int jj = 0; jj < 8; ++jj)                                                                 \
        w2r[i][jj] = __builtin_amdgcn_raw_buffer_load_b64(rw2, w2v + (jj & 3) * 1024, w2s + KS2(i) * 32768 + (jj >> 2) * 4096, 0); \
  }
  const v4u rw1 = {(u32)(unsigned long long)w1, (u32)((unsigned long long)w1 >> 32), (u32)(D * F * 2), 0x00020000u};
#define W1_DMA(ks) dma_w1x6(rw1, dvo, (32 * (ks) * F + f0) * 2, (u32)(uintptr_t)(LAS unsigned char*)smem + L_W1 + (ks) * 6144)
#define WAIT_LDS(idx, val)                                   \
  _Pragma("unroll 1") for (u32 sp = 0; sp < SPIN_CAP; ++sp) { \
    CFENCE();                                                \
    if (*(volatile LAS u32*)(lcnt + (idx)) >= (val)) break;  \
    __builtin_amdgcn_s_sleep(1);                             \
  }                                                          \
  CFENCE();
  f32x4* red = (f32x4*)(smem + L_RED);
  v4u fin;
#define FIN_LD() fin = LD128((lane >> 1) * 128 + (lane & 1) * 16, WS_FIN + w * 4096)
#define SIG_LDS(idx)                                    \
  asm volatile("s_waitcnt lgkmcnt(0)" ::: "memory"); \
  if (lane == 0) __hip_atomic_fetch_add(lcnt + (idx), 1u, __ATOMIC_RELAXED, __HIP_MEMORY_SCOPE_WORKGROUP);

  u32 pay = 0, scp[4] = {0, 0, 0, 0};
  asm volatile("s_waitcnt vmcnt(4)" : "+v"(L[0]), "+v"(L[1]), "+v"(L[2]), "+v"(L[3]), "+v"(Le)::"memory");
  asm volatile("" : "+s"(tag), "+s"(dd));
  if (w >= 6) {
      ST32(0u, tid == 384 ? 0 : 0x7fff0000, WS_CNT1 + b * 256);
      f32x2 e[4][4];
      float sm[4];
#pragma unroll
      for (int r = 0; r < 4; ++r) {
#pragma unroll
        for (int q = 0; q < 4; ++q) e[r][q] = bf_pair(L[r][q]);
        const f32x2 t = (e[r][0] + e[r][1]) + (e[r][2] + e[r][3]);
        sm[r] = t[0] + t[1];
      }
      const float m = row_sum(fold16(fold32(sm[0], sm[2]), fold32(sm[1], sm[3]))) * (1.f / D);
#pragma unroll
      for (int r = 0; r < 4; ++r) {
        const f32x2 mr = splat(rdlane(m, 16 * r));
        f32x2 t = {0.f, 0.f};
#pragma unroll
        for (int q = 0; q < 4; ++q) {
          const f32x2 dlt = e[r][q] - mr;
          t = fma2(dlt, dlt, t);
        }
        sm[r] = t[0] + t[1];
      }
      const float var = row_sum(fold16(fold32(sm[0], sm[2]), fold32(sm[1], sm[3]))) * (1.f / D);
      pay = pack_bf2(m, __builtin_amdgcn_sqrtf(rbf(rbf(var) + 1e-5f)));
  } else if (w >= 4) {
#pragma unroll
      for (int i = 0; i < 4; ++i) {
        f32x2 sc = bf_pair(Le[i]);
#pragma unroll
        for (int e = 0; e < 2; ++e) sc[e] = sc[e] * __builtin_amdgcn_rcpf(1.f + __expf(-sc[e]));
        scp[i] = pack2(sc);
      }
  } else {
    if (lane < 28) *(v4u*)(smem + L_U + w * C_UCOPY + 4096 + lane * 16) = v4u{0, 0, 0, 0};
    unsigned char* kp_ = smem + L_RED + 16384 + w * 4096 + 384 + (lane >> 3) * 256 + co * 16;
    *(v4u*)kp_ = L[0];
    *(v4u*)(kp_ + 128) = L[1];
  }
  asm volatile("s_waitcnt vmcnt(0)" : "+v"(L[4]), "+v"(L[5]), "+v"(L[6]), "+v"(L[7])::"memory");

  if (mainw) {
    v4u kr[5], sk[4];
    const v4u vr[2] = {L[2], L[3]};
    {
      unsigned char* sp = smem + L_RED + 16384 + w * 4096;
      if (lane < 24) *(v4u*)(sp + lane * 16) = L[6];
      *(v4u*)(sp + 2560 + lane * 16) = L[7];
#pragma unroll
      for (int i = 0; i < 3; ++i) kr[i] = *(const v4u*)(sp + (lane >> 3) * 256 + co * 16 + i * 128);
#pragma unroll
      for (int i = 0; i < 4; ++i) sk[i] = *(const v4u*)(sp + 2560 + co * 64 + i * 16);
      kr[3] = L[0];
      kr[4] = L[1];
    }
    {
      unsigned char* tp = smem + L_RED + w * 4096;
      const int dq = 7 - (lane >> 3);
#pragma unroll
      for (int q = 0; q < 4; ++q)
        *(v2u*)(tp + co * 512 + q * 128 + (((4 + (lane >> 4)) ^ co) << 4) + ((lane >> 3) & 1) * 8) = v2u{L[4][q], L[5][q]};
#pragma unroll
      for (int j = 0; j < 8; ++j) {
        const u32 s01 = sk[j >> 1][(j & 1) * 2], s23 = sk[j >> 1][(j & 1) * 2 + 1];
        u32 p[4];
#pragma unroll
        for (int i = 0; i < 4; ++i) p[i] = (j & 1) ? perm_hi(kr[i][j >> 1], kr[i + 1][j >> 1]) : perm_lo(kr[i][j >> 1], kr[i + 1][j >> 1]);
        const float ks0v = dot2(p[2], s01, dot2(p[0], s23, 0.f));
        const float ks1v = dot2(p[3], s01, dot2(p[1], s23, 0.f));
        const float v0 = (j & 1) ? bf_hi(vr[0][j >> 1]) : bf_lo(vr[0][j >> 1]);
        const float v1 = (j & 1) ? bf_hi(vr[1][j >> 1]) : bf_lo(vr[1][j >> 1]);
        *(u32*)(tp + co * 512 + (j >> 1) * 128 + (((2 * (j & 1) + (dq >> 2)) ^ co) << 4) + (dq & 3) * 4) = pack2(f32x2{ks1v, ks0v} * f32x2{v1, v0});
      }
      STAMP(1);
#pragma unroll
      for (int i = 0; i < 4; ++i) {
        const int q = lane + 64 * i;
        const v4u o = *(const v4u*)(tp + ((q ^ (q >> 5)) << 4));
        ST128(o, lane * 16 + 1024 * i, WS_KVR + (grp * 128 + 127 - 4 * mem - w) * KV_STR);
      }
    }
    STAMP(2);
    DRAIN();
    if (lane == 0) {
      if (__hip_atomic_fetch_add(lcnt + 6, 1u, __ATOMIC_RELAXED, __HIP_MEMORY_SCOPE_WORKGROUP) == 3u) ST32(tag, 0, WS_FLAG0 + (grp * 32 + mem) * 128);
    }
    STAMP(3);
    const int ch = w >> 1, hf = w & 1;
    const LAS u32* hb4[4];
#pragma unroll
    for (int i = 0; i < 4; ++i) {
      hb4[i] = (const LAS u32*)(smem + L_H + ch * C_HCH) + (c + 4 * g + 1 + 64 * hf) + 256 * i;
      asm volatile("" : "+v"(hb4[i]));
    }
    const LAS unsigned char* ub = (const LAS unsigned char*)smem + L_U + ch * C_UCH + (c & 1) * C_UCOPY + 2 * (2016 - 1792 - 32 * (c >> 1) + 8 * g) + 256 * hf;
    asm volatile("" : "+v"(ub));
    {
      int pm = 31 - 8 * w - (lane < 9 ? lane : 0);
      pm = pm < 0 ? 0 : pm;
      __builtin_amdgcn_s_sleep(MSLEEP0);
#pragma unroll 1
      for (u32 sp = 0; sp < SPIN_CAP; ++sp) {
        CFENCE();
        const u32 f = LD32(pm * 128, WS_FLAG0 + grp * 4096);
        if (__all(f == tag)) break;
        __builtin_amdgcn_s_sleep(MSLEEP);
      }
      CFENCE();
    }
    STAMP(4);
    {
      const int so = WS_KVR + (grp * 128 + 32 * w) * KV_STR + mem * 128;
      const int vo = (lane >> 2) * KV_STR + (lane & 3) * 16;
      const int vn = (lane & 1) ? vo + KV_STR - 16 : vo + 16;
      v4u uv[2];
      u32 un[2];
#pragma unroll
      for (int i = 0; i < 2; ++i) {
        uv[i] = LD128(vo, so + i * 16 * KV_STR);
        un[i] = LD32(vn, so + i * 16 * KV_STR);
      }
      if (lane == 0) __hip_atomic_fetch_add(lcnt + 2, 1u, __ATOMIC_RELAXED, __HIP_MEMORY_SCOPE_WORKGROUP);
      unsigned char* ud = smem + L_U + ((lane >> 1) & 1) * C_UCH + w * 1024 + (lane >> 2) * 32 + (lane & 1) * 16;
      if (tid >= 253 && (lane & 1)) un[1] = 0u;
#pragma unroll
      for (int i = 0; i < 2; ++i) {
        *(v4u*)(ud + i * 512 + C_UCOPY) = uv[i];
        v4u sh;
        sh[0] = __builtin_amdgcn_alignbit(uv[i][1], uv[i][0], 16);
        sh[1] = __builtin_amdgcn_alignbit(uv[i][2], uv[i][1], 16);
        sh[2] = __builtin_amdgcn_alignbit(uv[i][3], uv[i][2], 16);
        sh[3] = __builtin_amdgcn_alignbit(un[i], uv[i][3], 16);
        *(v4u*)(ud + i * 512) = sh;
      }
      asm volatile("s_waitcnt lgkmcnt(0)" ::: "memory");
      if (lane == 0) __hip_atomic_fetch_add(lcnt, 1u, __ATOMIC_RELAXED, __HIP_MEMORY_SCOPE_WORKGROUP);
    }
    WAIT_LDS(0, 6u);
    STAMP(5);
    __builtin_amdgcn_s_setprio(2);
    f32x4 acc[8];
#pragma unroll
    for (int n = 0; n < 8; ++n) acc[n] = f32x4{0.f, 0.f, 0.f, 0.f};
    v4u hfr[2][8], ufr[2][8];
#define CONV_LD(buf, rr, s)                                                                                            \
  {                                                                                                                    \
    ufr[buf][s] = *(const LAS v4u*)(ub + 64 * (rr) + 512 * (7 - (s)));                                                 \
    _Pragma("unroll") for (int jj = 0; jj < 4; ++jj) hfr[buf][s][jj] = hb4[(s) >> 1][16 * (rr) + 128 * ((s) & 1) + jj]; \
  }
#pragma unroll
    for (int s = 0; s < 4; ++s) CONV_LD(0, 0, s);
    SB();
#pragma unroll
    for (int rr = 0; rr < 4; ++rr) {
#pragma unroll
      for (int n = 0; n < 8; ++n) {
        if (rr == 0 && n >= 1 && n < 4) CONV_LD(0, 0, n + 3);
        if (rr == 0 && n == 3) CONV_LD(0, 0, 7);
        if (rr < 3 && n >= 4) {
          CONV_LD((rr + 1) & 1, rr + 1, 2 * (n - 4));
          CONV_LD((rr + 1) & 1, rr + 1, 2 * (n - 4) + 1);
        }
        if (rr == 3 && n > 0) red[(w * 8 + n - 1) * 64 + lane] = acc[n - 1];
#pragma unroll
        for (int s = 0; s <= n; ++s) acc[n] = mfma16(ufr[rr & 1][n - s], hfr[rr & 1][s], acc[n]);
#pragma unroll
        for (int s = 0; s <= n; ++s) {
          __builtin_amdgcn_sched_group_barrier(0x8, 1, 0);
          __builtin_amdgcn_sched_group_barrier(0x80, 1, 0);
        }
        SB();
      }
    }
    red[(w * 8 + 7) * 64 + lane] = acc[7];
    __builtin_amdgcn_s_setprio(0);
    SIG_LDS(4);
    STAMP(6);
    FIN_LD();
    WAIT_LDS(5, 4u);
    WAIT_LDS(4, 4u);
  } else {
   if (w < 6) {
    float a[8];
#pragma unroll
    for (int j = 0; j < 8; ++j) {
      a[j] = 0.f;
#pragma unroll
      for (int i = 0; i < 4; ++i)
        a[j] = dot2((j & 1) ? perm_hi(L[2 * i + 1][j >> 1], L[2 * i][j >> 1]) : perm_lo(L[2 * i + 1][j >> 1], L[2 * i][j >> 1]), scp[i], a[j]);
    }
    float r[2];
#pragma unroll
    for (int j = 0; j < 2; ++j) r[j] = dpp_add<0x128>(fold16(fold32(a[j], a[4 + j]), fold32(a[2 + j], a[6 + j])));
    const int src = ((lane & 3) << 4) + ((lane >> 2) & 7);
    const float r0 = __shfl(r[0], src), r1 = __shfl(r[1], src);
    ST128((v4u{__builtin_bit_cast(u32, r0), tag, __builtin_bit_cast(u32, r1), tag}), (mem < 24 && lane < 32) ? lane * 16 : 0x7fff0000,
          WS_MODP + ((kq * 2 + wl) * 3072 + mq * 512 + grp * 64) * 8);
    STAMP(2);
   } else {
      const int e = w - 6;
      const u32 p1 = __builtin_amdgcn_readlane(pay, 16), p2 = __builtin_amdgcn_readlane(pay, 32), p3 = __builtin_amdgcn_readlane(pay, 48);
      ST128((v4u{lane ? p2 : pay, tag, lane ? p3 : p1, tag}), lane < 2 ? lane * 16 : 0x7fff0000, WS_ST1 + (8 * b + 4 * e) * 8);
      STAMP(2);
      unsigned char* hd = smem + L_H + e * C_HCH;
      if (lane < 4) *(v4u*)(hd + lane * 16) = v4u{0, 0, 0, 0};
#pragma unroll
      for (int i = 0; i < 4; ++i) *(v4u*)(hd + 64 + lane * 16 + i * 1024) = L[4 + i];
      SIG_LDS(0);
   }
    u32 dvo[6];
#pragma unroll
    for (int n = 0; n < 6; ++n) {
      const int i = 64 * n + lane, row = i / 12, slot = i % 12;
      dvo[n] = row * (F * 2) + (slot < noct ? slot : 0) * 16;
    }
    const int tb = 512 * wl + 64 * g + 2 * c;
    const int tb4 = tb * 4, tb8 = tb * 8;
    v4u stv[4];
    v2u mp[2], xv[4];
    const int mvo = ((g * 2 * 3072) + ((c < 12 ? c : 0) >> 1) * 512 + (c & 1)) * 8;
    {
      const v4u rws = {(u32)(unsigned long long)ws, (u32)((unsigned long long)ws >> 32), (u32)WS_END, 0x00020000u};
      int xo = (13 - 4 * g - (c >> 3)) * KV_STR + (c & 7) * 8, so = tb8 + WS_ST1, mo = mvo;
      const int xs = WS_KVR + (grp * 128 + 96 - 32 * wl) * KV_STR + mem * 128 + 64;
      asm volatile("" : "+v"(xo), "+v"(so), "+v"(mo), "+v"(dvo[0]), "+v"(dvo[1]), "+v"(dvo[2]), "+v"(dvo[3]), "+v"(dvo[4]), "+v"(dvo[5]));
      STAMP(1);
      WAIT_LDS(2, 4u);
      asm volatile(
          "buffer_load_dwordx4 %0, %10, %13, 0 offen sc1\n\tbuffer_load_dwordx4 %1, %10, %13, 0 offen offset:256 sc1\n\t"
          "buffer_load_dwordx4 %2, %10, %13, 0 offen offset:2048 sc1\n\tbuffer_load_dwordx4 %3, %10, %13, 0 offen offset:2304 sc1\n\t"
          "buffer_load_dwordx2 %4, %11, %13, %14 offen sc1\n\tbuffer_load_dwordx2 %5, %11, %13, %15 offen sc1\n\t"
          "buffer_load_dwordx2 %6, %12, %13, %16 offen sc1\n\tbuffer_load_dwordx2 %7, %12, %13, %17 offen sc1\n\t"
          "buffer_load_dwordx2 %8, %12, %13, %18 offen sc1\n\tbuffer_load_dwordx2 %9, %12, %13, %19 offen sc1"
          : "=&v"(stv[0]), "=&v"(stv[1]), "=&v"(stv[2]), "=&v"(stv[3]), "=&v"(mp[0]), "=&v"(mp[1]), "=&v"(xv[0]), "=&v"(xv[1]), "=&v"(xv[2]), "=&v"(xv[3])
          : "v"(so), "v"(mo), "v"(xo), "s"(rws), "s"(WS_MODP + c0 * 8), "s"(WS_MODP + (3072 + c0) * 8), "s"(xs + 18 * KV_STR), "s"(xs + 16 * KV_STR),
            "s"(xs + 2 * KV_STR), "s"(xs));
    }
    __builtin_amdgcn_s_sleep(DSLEEP);
    if (w == 4) {
      DRAIN();
    } else {
      W1_DMA(2 * wl);
      W1_DMA(2 * wl + 1);
    }
    asm volatile("s_waitcnt vmcnt(12) ; operands"
                 : "+v"(stv[0]), "+v"(stv[1]), "+v"(stv[2]), "+v"(stv[3]), "+v"(mp[0]), "+v"(mp[1]), "+v"(xv[0]), "+v"(xv[1]), "+v"(xv[2]), "+v"(xv[3])::"memory");
    STAMP(4);
#define GR_BAD() ((mp[0][1] ^ tag) | (mp[1][1] ^ tag) | (stv[0][1] ^ tag) | (stv[0][3] ^ tag) | (stv[1][1] ^ tag) | (stv[1][3] ^ tag) | \
                  (stv[2][1] ^ tag) | (stv[2][3] ^ tag) | (stv[3][1] ^ tag) | (stv[3][3] ^ tag))
    if (!__all(GR_BAD() == 0)) {
#pragma unroll 1
      for (u32 sp = 0; sp < SPIN_CAP; ++sp) {
        CFENCE();
#pragma unroll
        for (int i = 0; i < 4; ++i) stv[i] = LD128(tb8 + (256 * (i >> 1) + 32 * (i & 1)) * 8, WS_ST1);
#pragma unroll
        for (int i = 0; i < 2; ++i) mp[i] = LD64(mvo, WS_MODP + (i * 3072 + c0) * 8);
        if (__all(GR_BAD() == 0)) break;
        __builtin_amdgcn_s_sleep(PSLEEP);
      }
    }
    float md = __builtin_bit_cast(float, mp[0][0]) + __builtin_bit_cast(float, mp[1][0]);
    md = fold16(md, md);
    md = rbf(fold32(md, md));
    if (w == 4) {
      const float sbv = lane ? rdlane(md, 7) : rdlane(md, 6), cbv = lane ? rdlane(md, 9) : rdlane(md, 8), gbv = lane ? rdlane(md, 11) : rdlane(md, 10);
      ST128((v4u{pack_bf2(1.f + cbv, sbv), tag, __builtin_bit_cast(u32, gbv), tag}), lane < 2 ? lane * 16 : 0x7fff0000, WS_FIN + pair * 128);
      CFENCE();
      W1_DMA(0);
      W1_DMA(1);
    }
    const f32x2 sa = {rdlane(md, 0), rdlane(md, 1)}, ga = {rdlane(md, 4), rdlane(md, 5)};
    const f32x2 ca = {rbf(1.f + rdlane(md, 2)), rbf(1.f + rdlane(md, 3))};
    const f32x2 gca = ga * ca, gsa = ga * sa, dv = bf_pair(dd);
    W1_DMA(2 * w);
    WAIT_LDS(0, 6u);
    STAMP(5);
    f32x2 P[4][2], Q[4][2];
    const unsigned char* u1a = smem + L_U + C_UCOPY + (2046 - 288 - tb) * 2;
#pragma unroll
    for (int i = 0; i < 4; ++i) {
      const int o = (288 - 256 * (i >> 1) - 32 * (i & 1)) * 2;
      const u32 kva = *(const u32*)(u1a + o), kvb = *(const u32*)(u1a + C_UCH + o);
#pragma unroll
      for (int r = 0; r < 2; ++r) {
        const f32x2 kv = r ? f32x2{bf_lo(kva), bf_lo(kvb)} : f32x2{bf_hi(kva), bf_hi(kvb)};
        const f32x2 xx = bf_pair(xv[i][r]);
        const u32 sw = stv[i][2 * r];
        P[i][r] = fma2(xx - splat(bf_lo(sw)), splat(__builtin_amdgcn_rcpf(bf_hi(sw))) * gca, gsa);
        Q[i][r] = fma2(P[i][r], kv * dv, xx);
        asm volatile("" : "+v"(P[i][r]), "+v"(Q[i][r]));
      }
      if (i == 1) W1_DMA(2 * w + 1);
    }
    asm volatile("s_waitcnt vmcnt(12)" ::: "memory");
    SIG_LDS(5);
    STAMP(6);
    __builtin_amdgcn_s_setprio(3);
    WAIT_LDS(4, 4u);
#pragma unroll
    for (int i = 0; i < 2; ++i) {
      const int n = 2 * wl + i;
      const f32x4 y0 = red[(0 * 8 + n) * 64 + lane] + red[(1 * 8 + n) * 64 + lane];
      const f32x4 y1 = red[(2 * 8 + n) * 64 + lane] + red[(3 * 8 + n) * 64 + lane];
#pragma unroll
      for (int h = 0; h < 2; ++h) {
        v2u o;
#pragma unroll
        for (int r = 0; r < 2; ++r)
          o[r] = pack_bf2(__builtin_fmaf(P[2 * i + h][r][0], y0[2 * h + r], Q[2 * i + h][r][0]),
                          __builtin_fmaf(P[2 * i + h][r][1], y1[2 * h + r], Q[2 * i + h][r][1]));
        ST64(o, tb4 + (256 * i + 32 * h) * 4, WS_X1P + pair * XP_STR);
      }
    }
    FIN_LD();
    STAMP(7);
    DRAIN();
    u32 arr = 0;
    if (lane == 0) arr = __hip_atomic_fetch_add(lcnt + 1, 1u, __ATOMIC_RELAXED, __HIP_MEMORY_SCOPE_WORKGROUP);
    if (__builtin_amdgcn_readfirstlane(arr) == 3u && lane < 32)
      __hip_atomic_fetch_add((GAS u32*)(ws + WS_CNT1 + (lane * 8 + grp) * 256), 1u, __ATOMIC_RELAXED, __HIP_MEMORY_SCOPE_AGENT);
    __builtin_amdgcn_s_setprio(0);
  }
  STAMP(8);

  {
    float* csp = (float*)(smem + L_CSP);
    const u16 b1v = b1[f0 + ((tid >= 416 && tid - 416 < 8 * noct) ? tid - 416 : 0)];
    if (!__all(fin[1] == tag && fin[3] == tag)) {
#pragma unroll 1
      for (u32 sp = 0; sp < SPIN_CAP; ++sp) {
        __builtin_amdgcn_s_sleep(PSLEEP);
        CFENCE();
        FIN_LD();
        if (__all(fin[1] == tag && fin[3] == tag)) break;
      }
    }
    *(u16*)(smem + L_SA + tid * 2) = (u16)fin[0];
    *(u16*)(smem + L_SB + tid * 2) = (u16)(fin[0] >> 16);
    *(u16*)(smem + L_GB + tid * 2) = (u16)(fin[2] >> 16);
    STAMP(16);
    f32x4 a3[6];
#pragma unroll
    for (int j = 0; j < 6; ++j) a3[j] = f32x4{0.f, 0.f, 0.f, 0.f};
    const u32 dmask = g == (c >> 2) ? ((c & 1) ? 0xffff0000u : 0x0000ffffu) : 0u;
    const u32 m0 = (c & 2) ? 0u : dmask, m1 = (c & 2) ? dmask : 0u;
    const u32 m3 = c < 2 ? 0xffffffffu : 0u;
    const bool ones = (c >> 2) == 3;
    unsigned char* blk = smem + L_W1 + w * 12288;
    const unsigned char* trb = blk + (8 * g + (c >> 2)) * 192 + (c & 3) * 16;
    unsigned char* wrb = blk + (g >> 1) * 3072 + ((g & 1) * 32 + (c & 3) + 16 * ((c >> 2) & 1) + 4 * (c >> 3)) * 16;
    v4u raw[2][6], A1[2], A2[2], A3[2];
#pragma unroll
    for (int q = 0; q < 2; ++q) {
      const int kb = 64 * w + 32 * q + 8 * g;
      const v4u sv = *(const v4u*)(smem + L_SA + kb * 2);
      const v4u s3 = *(const v4u*)(smem + (c == 1 ? L_SB : L_SA) + kb * 2);
      A1[q] = v4u{sv[0] & m0, sv[1] & m1, 0u, 0u};
      A2[q] = v4u{0u, 0u, sv[2] & m0, sv[3] & m1};
      A3[q] = v4u{s3[0] & m3, s3[1] & m3, s3[2] & m3, s3[3] & m3};
#pragma unroll
      for (int j = 0; j < 6; ++j) {
        const unsigned char* p = trb + q * 6144 + (j >> 1) * 64 + (j & 1) * 8;
        const v2u lo = __builtin_bit_cast(v2u, __builtin_amdgcn_ds_read_tr16_b64_v4i16((LAS v4s*)p));
        const v2u hi = __builtin_bit_cast(v2u, __builtin_amdgcn_ds_read_tr16_b64_v4i16((LAS v4s*)(p + 4 * 192)));
        raw[q][j] = v4u{lo[0], lo[1], hi[0], hi[1]};
      }
    }
    CFENCE();
#pragma unroll
    for (int q = 0; q < 2; ++q)
#pragma unroll
      for (int j = 0; j < 6; ++j) {
        const f32x4 z = {0.f, 0.f, 0.f, 0.f};
        const f32x4 p1 = mfma16(A1[q], raw[q][j], z), p2 = mfma16(A2[q], raw[q][j], z);
        a3[j] = mfma16(A3[q], raw[q][j], a3[j]);
        v4u o = {pack_bf2(p1[0], p1[1]), pack_bf2(p1[2], p1[3]), pack_bf2(p2[0], p2[1]), pack_bf2(p2[2], p2[3])};
        if (j == 4 && ones) o = v4u{0x3f803f80u, 0x3f803f80u, 0x3f803f80u, 0x3f803f80u};
        *(v4u*)(wrb + q * 6144 + (j >> 1) * 1024 + (j & 1) * 128) = o;
      }
    if (g == 0) {
#pragma unroll
      for (int j = 0; j < 6; ++j) {
        const int fc = 32 * (j >> 1) + 8 * (c >> 2) + 4 * (j & 1) + (c & 3);
        csp[w * 192 + fc] = a3[j][0];
        csp[w * 192 + 96 + fc] = a3[j][1];
      }
    }
    STAMP(17);
    SIG_LDS(3);
    if (mainw) {
      WAIT_LDS(1, 4u);
#pragma unroll
      for (int i = 0; i < NW2C; ++i) W2_LOAD(i);
    } else if (w > 4) {
      WAIT_LDS(3, 8u);
      const int i = tid - 320;
      float cv = ((csp[i] + csp[192 + i]) + (csp[384 + i] + csp[576 + i])) + ((csp[768 + i] + csp[960 + i]) + (csp[1152 + i] + csp[1344 + i]));
      if (i >= 96) cv += bf2f(b1v);
      ((float*)(smem + L_CS))[i] = cv;
    }
    SB();
  }
  STAMP(9);
  if (w == 4) {
#pragma unroll 1
    for (u32 sp = 0; sp < SPIN_CAP; ++sp) {
      CFENCE();
      const u32 f = LD32((lane & 7) * 256, WS_CNT1 + (b & 31) * 2048);
      if (__all(f == 32u)) break;
    }
    if (b == 0 && lane == 0) ST32(tag, 0, WS_EPOCH);
  }
  BAR();
  STAMP(10);

  const int qe = w >> 1, he = w & 1, jp = lane >> 5, ge = 2 * he + ((lane >> 4) & 1);
  const int te = 64 * rt + 32 * (qe >> 1) + 4 * (c & 7) + (c >> 3) + 2 * (qe & 1);
  if (mainw) {
    __builtin_amdgcn_s_setprio(2);
    const int t0 = 128 * rp + 32 * w;
    v4u xl[32];
    const int xvo = (lane >> 3) * XP_STR + 16 * (lane & 7), xso = WS_X1P + t0 * 4;
#define X_LD(kk) xl[kk] = LD128(xvo, xso + 8 * (kk) * XP_STR)
    u32 xt0, xt2;
#define X_FRAG_A(kk, A, B, C, E)                                                                                     \
  asm volatile("v_permlane16_swap_b32 v" #A ", v" #C "\n\tv_permlane16_swap_b32 v" #B ", v" #E "\n\tv_mov_b32 %1, v" #A \
               "\n\tv_mov_b32 %2, v" #C                                                                               \
               : "+{v[" #A ":" #E "]}"(xl[kk]), "=&v"(xt0), "=&v"(xt2));
#define X_FRAG_B(kk, A, B, C, E)                                                                                     \
  asm volatile("v_mov_b32_dpp v" #A ", v" #B " row_ror:8 row_mask:0xf bank_mask:0xc\n\t"                              \
               "v_mov_b32_dpp v" #C ", v" #E " row_ror:8 row_mask:0xf bank_mask:0xc\n\t"                              \
               "v_mov_b32_dpp v" #B ", %1 row_ror:8 row_mask:0xf bank_mask:0x3\n\t"                                   \
               "v_mov_b32_dpp v" #E ", %2 row_ror:8 row_mask:0xf bank_mask:0x3"                                       \
               : "+{v[" #A ":" #E "]}"(xl[kk]) : "v"(xt0), "v"(xt2));
#pragma unroll
    for (int kk = 0; kk < PF1; ++kk) X_LD(kk);
    f32x16 acc[3];
#pragma unroll
    for (int j = 0; j < 3; ++j)
#pragma unroll
      for (int r = 0; r < 16; ++r) acc[j][r] = 0.f;
    float sq[2] = {0.f, 0.f};
    const LAS unsigned char* wfl[2] = {(const LAS unsigned char*)smem + L_W1 + lane * 16, (const LAS unsigned char*)smem + L_W1 + 49152 + lane * 16};
    asm volatile("" : "+v"(wfl[0]), "+v"(wfl[1]));
    v4u wf[2][3];
#define W_LD(kk) \
  { _Pragma("unroll") for (int j = 0; j < 3; ++j) wf[(kk) & 1][j] = *(const LAS v4u*)(wfl[(kk) >> 4] + (((kk) & 15) * 3 + j) * 1024); }
    W_LD(0);
    X_FRAG_A(0, 200, 201, 202, 203);
    X_FRAG_B(0, 200, 201, 202, 203);
    asm volatile("s_nop 1");
    SB();
#define G1_STEP(kk, A, B, C, E)                                                                 \
  {                                                                                             \
    acc[0] = mfma32(wf[(kk) & 1][0], xl[kk], acc[0]);                                           \
    SB();                                                                                       \
    if ((kk) + 1 < 32) {                                                                        \
      W_LD((kk) + 1);                                                                           \
      X_FRAG_A((kk) + 1 < 32 ? (kk) + 1 : 31, A, B, C, E);                                      \
    }                                                                                           \
    SB();                                                                                       \
    acc[1] = mfma32(wf[(kk) & 1][1], xl[kk], acc[1]);                                           \
    SB();                                                                                       \
    if ((kk) + 1 < 32) { X_FRAG_B((kk) + 1 < 32 ? (kk) + 1 : 31, A, B, C, E); }                 \
    if ((kk) + PF1 < 32) X_LD((kk) + PF1 < 32 ? (kk) + PF1 : 31);                               \
    SB();                                                                                       \
    acc[2] = mfma32(wf[(kk) & 1][2], xl[kk], acc[2]);                                           \
    SB();                                                                                       \
    _Pragma("unroll") for (int i = 0; i < 4; ++i) sq[i & 1] = dot2(xl[kk][i], xl[kk][i], sq[i & 1]); \
    asm volatile("" : "+v"(sq[0]), "+v"(sq[1]));                                                \
    if ((kk) == 31 - PF1) ((volatile LAS u32*)lcnt)[8 + w] = 1u;                                \
    SB();                                                                                       \
    if ((kk) == 0) STAMP(18);                                                                   \
    if ((kk) == 15) STAMP(19);                                                                  \
  }
    G1_STEP(0, 204, 205, 206, 207)
    G1_STEP(1, 208, 209, 210, 211)
    G1_STEP(2, 212, 213, 214, 215)
    G1_STEP(3, 216, 217, 218, 219)
    G1_STEP(4, 220, 221, 222, 223)
    G1_STEP(5, 224, 225, 226, 227)
    G1_STEP(6, 228, 229, 230, 231)
    G1_STEP(7, 232, 233, 234, 235)
    G1_STEP(8, 236, 237, 238, 239)
    G1_STEP(9, 240, 241, 242, 243)
    G1_STEP(10, 244, 245, 246, 247)
    G1_STEP(11, 200, 201, 202, 203)
    G1_STEP(12, 204, 205, 206, 207)
    G1_STEP(13, 208, 209, 210, 211)
    G1_STEP(14, 212, 213, 214, 215)
    G1_STEP(15, 216, 217, 218, 219)
    G1_STEP(16, 220, 221, 222, 223)
    G1_STEP(17, 224, 225, 226, 227)
    G1_STEP(18, 228, 229, 230, 231)
    G1_STEP(19, 232, 233, 234, 235)
    G1_STEP(20, 236, 237, 238, 239)
    G1_STEP(21, 240, 241, 242, 243)
    G1_STEP(22, 244, 245, 246, 247)
    G1_STEP(23, 200, 201, 202, 203)
    G1_STEP(24, 204, 205, 206, 207)
    G1_STEP(25, 208, 209, 210, 211)
    G1_STEP(26, 212, 213, 214, 215)
    G1_STEP(27, 216, 217, 218, 219)
    G1_STEP(28, 220, 221, 222, 223)
    G1_STEP(29, 224, 225, 226, 227)
    G1_STEP(30, 228, 229, 230, 231)
    G1_STEP(31, 232, 233, 234, 235)
    STAMP(11);
    const float* cs = (const float*)(smem + L_CS) + 16 * jp;
    float sum = acc[2][8], ssq = sq[0] + sq[1];
    {
      const auto sw = __builtin_amdgcn_permlane32_swap(__builtin_bit_cast(u32, sum), __builtin_bit_cast(u32, sum), false, false);
      sum = __builtin_bit_cast(float, (u32)sw[1]);
      ssq = fold32(ssq, ssq);
    }
    const float m = sum * (1.f / D);
    const float var = fmaxf(__builtin_fmaf(-m, m, ssq * (1.f / D)), 0.f);
    const float rsd = __builtin_amdgcn_rcpf(rbf(__builtin_amdgcn_sqrtf(rbf(rbf(var) + 1e-5f))));
    const f32x2 rs2 = splat(rsd), mr2 = splat(-rbf(m) * rsd);
#pragma unroll
    for (int o8 = 0; o8 < 6; ++o8) {
      const int j = o8 >> 1, r0 = 8 * (o8 & 1);
      v4u o;
#pragma unroll
      for (int h = 0; h < 2; ++h) {
        const f32x4 u1 = *(const f32x4*)(cs + 32 * j + r0 + 4 * h), s1 = *(const f32x4*)(cs + 96 + 32 * j + r0 + 4 * h);
#pragma unroll
        for (int q = 0; q < 2; ++q) {
          const int r = r0 + 4 * h + 2 * q;
          f32x2 a = fma2(f32x2{acc[j][r], acc[j][r + 1]}, rs2, fma2(f32x2{u1[2 * q], u1[2 * q + 1]}, mr2, f32x2{s1[2 * q], s1[2 * q + 1]}));
          a = __builtin_elementwise_max(a, f32x2{0.f, 0.f});
          o[2 * h + q] = pack2(a * a);
        }
      }
      const int slot = 4 * j + (o8 & 1);
      ST128(o, (j < 2 || slot + 2 * jp < noct) ? 2 * jp * HO_STR + (lane & 31) * 16 : 0x7fff0000, WS_HO + ((f0 >> 3) + slot) * HO_STR + t0 * 16);
    }
    STAMP(22);
    DRAIN();
    STAMP(23);
    if (lane == 0) {
      if (__hip_atomic_fetch_add(lcnt + 7, 1u, __ATOMIC_RELAXED, __HIP_MEMORY_SCOPE_WORKGROUP) == 3u) ST32(tag, 0, WS_FLAG2 + (rp * 16 + ct1) * 128);
    }
    __builtin_amdgcn_s_setprio(0);
#pragma unroll
    for (int i = NW2C; i < 6; ++i) W2_LOAD(i);
  } else {
#pragma unroll 1
    for (u32 sp = 0; sp < SPIN_CAP; ++sp) {
      CFENCE();
      const v4u f = *(volatile LAS v4u*)(lcnt + 8);
      if ((f[0] & f[1] & f[2] & f[3]) != 0u) break;
      __builtin_amdgcn_s_sleep(2);
    }
#pragma unroll
    for (int i = 0; i < 5; ++i) W2_LOAD(i);
    STAMP(11);
  }
  STAMP(12);
  u32 x1v[4], b2v[4];
#pragma unroll
  for (int i = 0; i < 4; ++i) {
    x1v[i] = LD32((8 * ge + jp) * XP_STR + te * 4, WS_X1P + (32 * ct2 + 2 * i) * XP_STR);
    b2v[i] = *(const GAS u32*)(b2 + 64 * ct2 + 16 * ge + 2 * jp + 4 * i);
  }
  SB();
  v4u wf2[6][4];
#pragma unroll
  for (int i = 0; i < 6; ++i)
#pragma unroll
    for (int j = 0; j < 4; ++j) {
#pragma unroll
      for (int p = 0; p < 4; ++p)
        wf2[i][j][p] = (j & 1) ? perm_hi(w2r[i][2 * p + 1][j >> 1], w2r[i][2 * p][j >> 1]) : perm_lo(w2r[i][2 * p + 1][j >> 1], w2r[i][2 * p][j >> 1]);
      asm volatile("" : "+v"(wf2[i][j]));
    }
  SB();
  STAMP(20);
  if (w == 7) {
    WAIT_LDS(7, 4u);
#pragma unroll 1
    for (u32 sp = 0; sp < SPIN_CAP; ++sp) {
      CFENCE();
      const u32 f = LD32(c * 128, WS_FLAG2 + (rt >> 1) * 2048);
      if (__all(f == tag)) break;
    }
  }
  BAR();
  STAMP(13);

  {
    v4u hf[2][6][2];
    const int hvo = g * HO_STR + c * 16, hso = WS_HO + 4 * ks0 * HO_STR + 64 * rt * 16;
#define H_LD(ps, i)                                                                                              \
  if ((i) < 5 || w < 3) {                                                                                        \
    _Pragma("unroll") for (int q = 0; q < 2; ++q) hf[ps][i][q] = LD128(hvo + (2 * (ps) + q) * 256, hso + KS2(i) * 4 * HO_STR); \
  }
#pragma unroll
    for (int i = 0; i < 4; ++i) H_LD(0, i);
    f32x4* red2 = (f32x4*)smem;
    SB();
#pragma unroll
    for (int ps = 0; ps < 2; ++ps) {
      f32x4 acc[4][2];
#pragma unroll
      for (int j = 0; j < 4; ++j)
#pragma unroll
        for (int q = 0; q < 2; ++q) acc[j][q] = f32x4{0.f, 0.f, 0.f, 0.f};
#pragma unroll
      for (int i = 0; i < 6; ++i) {
        if (6 * ps + i + 4 < 12) H_LD((6 * ps + i + 4) / 6, (6 * ps + i + 4) % 6);
        if (i < 5 || w < 3) {
#pragma unroll
          for (int q = 0; q < 2; ++q)
#pragma unroll
            for (int j = 0; j < 4; ++j) acc[j][q] = mfma16(wf2[i][j], hf[ps][i][q], acc[j][q]);
        }
        SB();
        if (ps == 0 && i == 0) STAMP(24);
      }
      if (ps == 0) STAMP(25); else STAMP(26);
#pragma unroll
      for (int q = 0; q < 2; ++q)
#pragma unroll
        for (int j = 0; j < 4; ++j) red2[((w * 4 + 2 * ps + q) * 4 + j) * 64 + lane] = acc[j][q];
      SB();
    }
    STAMP(14);
    BAR();
    STAMP(27);
    f32x4 a[2];
    const f32x4* rd = (const f32x4*)smem + (qe * 4 + 2 * jp) * 64 + 32 * he + (lane & 31);
#pragma unroll
    for (int j = 0; j < 2; ++j) {
      f32x4 p[8];
#pragma unroll
      for (int ww = 0; ww < 8; ++ww) p[ww] = rd[(ww * 16 + j) * 64];
      a[j] = ((p[0] + p[1]) + (p[2] + p[3])) + ((p[4] + p[5]) + (p[6] + p[7]));
    }
    STAMP(28);
    u32 o[4];
#pragma unroll
    for (int r = 0; r < 4; ++r) {
      const u32 gw = *(const u32*)(smem + L_GB + (64 * ct2 + 16 * ge + 2 * jp + 4 * r) * 2);
      o[r] = pack2(fma2(bf_pair(gw), f32x2{a[0][r], a[1][r]} + bf_pair(b2v[r]), bf_pair(x1v[r])));
    }
    const auto s02 = __builtin_amdgcn_permlane32_swap(o[0], o[2], false, false);
    const auto s13 = __builtin_amdgcn_permlane32_swap(o[1], o[3], false, false);
    *(GAS v4u*)(out + (size_t)te * D + 64 * ct2 + 16 * ge + 8 * jp) = v4u{(u32)s02[0], (u32)s02[1], (u32)s13[0], (u32)s13[1]};
  }
}

extern "C" void kernel_launch(void* const* d_in, const int* in_sizes, int n_in, void* d_out, int out_size, void* d_ws, size_t ws_size,
                              hipStream_t stream) {
  (void)hipFuncSetAttribute((const void*)mega, hipFuncAttributeMaxDynamicSharedMemorySize, L_TOT);
  Params p;
  const void* r0[5] = {d_in[1], d_in[2], d_in[0], d_in[3], d_in[1]};
  const void* r1[5] = {d_in[12], d_in[12], d_in[12], d_in[12], d_in[12]};
  const void* r2[5] = {d_in[0], d_in[0], d_in[4], d_in[4], d_in[4]};
  const void* rr[8] = {d_in[11], d_ws, d_in[5], d_in[7], d_in[8], d_in[9], d_in[10], d_out};
  p.pad = d_ws;
  for (int i = 0; i < 5; ++i) {
    p.role[0][i] = r0[i];
    p.role[1][i] = r1[i];
    p.role[2][i] = r2[i];
  }
  for (int i = 0; i < 8; ++i) p.rest[i] = rr[i];
  mega<<<256, 512, L_TOT, stream>>>(p);
}
```

```cpp
#include <hip/hip_runtime.h>
#include <hip/hip_bf16.h>
#include <stdint.h>

typedef unsigned int u32;
typedef unsigned short u16;
typedef u32 v4u __attribute__((ext_vector_type(4)));
typedef u32 v2u __attribute__((ext_vector_type(2)));
typedef float f32x4 __attribute__((ext_vector_type(4)));
typedef float f32x2 __attribute__((ext_vector_type(2)));
typedef __bf16 bf16x8 __attribute__((ext_vector_type(8)));
typedef __bf16 bf16x2 __attribute__((ext_vector_type(2)));
typedef short v4s __attribute__((ext_vector_type(4)));
#define LAS __attribute__((address_space(3)))
#define GAS __attribute__((address_space(1)))

constexpr int S = 2048, D = 512, F = 1376;

constexpr int WS_EPOCH = 0;
constexpr int WS_FLAG0 = 4096;
constexpr int WS_FLAG2 = WS_FLAG0 + 256 * 128;
constexpr int WS_CNT1 = WS_FLAG2 + 512 * 128;
constexpr int WS_ST1 = WS_CNT1 + 256 * 256;
constexpr int WS_FIN = WS_ST1 + S * 8;
constexpr int WS_MODP = WS_FIN + 256 * 128;
constexpr int KV_STR = 4096 + 128;
constexpr int XP_STR = S * 4 + 128;
constexpr int HO_STR = S * 16 + 256;
constexpr int WS_KVR = 1 << 20;
constexpr int WS_X1P = WS_KVR + 1025 * KV_STR + 128;
constexpr int WS_HO = WS_X1P + 256 * XP_STR;
constexpr int WS_RS2 = WS_HO + (F / 8) * HO_STR;
constexpr int WS_END = WS_RS2 + S * 4;
static_assert(WS_X1P % 128 == 0 && WS_MODP + 8 * 3072 * 8 <= WS_KVR, "ctrl region");

constexpr int L_W1 = 0;
constexpr int C_HCH = 64 + 4096;
constexpr int C_UCOPY = 4544 + 32;
constexpr int C_UCH = 2 * C_UCOPY;
constexpr int L_H = 98304, L_U = L_H + 2 * C_HCH, L_RED = L_U + 2 * C_UCH, L_MISC = L_RED + 32768;
constexpr int L_SA = L_MISC, L_SB = L_SA + 1024, L_GB = L_SB + 1024, L_CS = L_GB + 1024, L_CNT = L_CS + 768, L_END = L_CNT + 128;
constexpr int L_CSP = L_H;
constexpr int L_CSF = L_H + 6144;
static_assert(L_U % 16 == 0 && L_RED % 16 == 0 && L_END <= 163840, "lds");

constexpr u32 SPIN_CAP = 1u << 18;
#define MSLEEP0 8
#define MSLEEP 4
#define DSLEEP 2
#define BNT ""
#define PF1 10
#define LATEW 1
#define KSIG 14
#define G2PRIO 1
#define PF2 3
#define FIN_STR 32
#define NM 5
#define PF2M 3
#define MAP1 0
#define MAP2 0
#define PSLEEP 10

#define L_TOT L_END
#define STAMP(i)

__device__ __forceinline__ float bf_lo(u32 v) { return __builtin_bit_cast(float, v << 16); }
__device__ __forceinline__ float bf_hi(u32 v) { return __builtin_bit_cast(float, v & 0xffff0000u); }
__device__ __forceinline__ f32x2 bf_pair(u32 v) { return f32x2{bf_lo(v), bf_hi(v)}; }
__device__ __forceinline__ float bf2f(u16 v) { return __builtin_bit_cast(float, (u32)v << 16); }
__device__ __forceinline__ u32 pack2(f32x2 v) { return __builtin_bit_cast(u32, __builtin_convertvector(v, bf16x2)); }
__device__ __forceinline__ u32 pack_bf2(float lo, float hi) { return pack2(f32x2{lo, hi}); }
__device__ __forceinline__ float rbf(float x) { return bf_lo(pack_bf2(x, 0.f)); }
__device__ __forceinline__ f32x2 fma2(f32x2 a, f32x2 b, f32x2 c) { return __builtin_elementwise_fma(a, b, c); }
__device__ __forceinline__ f32x2 splat(float a) { return f32x2{a, a}; }
__device__ __forceinline__ float dot2(u32 a, u32 b, float c) {
  return __builtin_amdgcn_fdot2_f32_bf16(__builtin_bit_cast(bf16x2, a), __builtin_bit_cast(bf16x2, b), c, false);
}
__device__ __forceinline__ u32 perm_lo(u32 hi, u32 lo) { return __builtin_amdgcn_perm(hi, lo, 0x05040100u); }
__device__ __forceinline__ u32 perm_hi(u32 hi, u32 lo) { return __builtin_amdgcn_perm(hi, lo, 0x07060302u); }
__device__ __forceinline__ f32x4 mfma16(v4u a, v4u b, f32x4 c) {
  return __builtin_amdgcn_mfma_f32_16x16x32_bf16(__builtin_bit_cast(bf16x8, a), __builtin_bit_cast(bf16x8, b), c, 0, 0, 0);
}
typedef float f32x16 __attribute__((ext_vector_type(16)));
__device__ __forceinline__ f32x16 mfma32(v4u a, v4u b, f32x16 c) {
  return __builtin_amdgcn_mfma_f32_32x32x16_bf16(__builtin_bit_cast(bf16x8, a), __builtin_bit_cast(bf16x8, b), c, 0, 0, 0);
}
__device__ __forceinline__ f32x4 mfma_sq(u32 a, u32 b, f32x4 c) {
  const v4s v = __builtin_bit_cast(v4s, v2u{a, b});
  return __builtin_amdgcn_mfma_f32_4x4x4bf16_1k(v, v, c, 0, 0, 0);
}
template <int CTRL> __device__ __forceinline__ float dpp_add(float v) {
  return v + __builtin_bit_cast(float, __builtin_amdgcn_update_dpp(0, __builtin_bit_cast(int, v), CTRL, 0xf, 0xf, true));
}
__device__ __forceinline__ float fold32(float a, float b) {
  auto r = __builtin_amdgcn_permlane32_swap(__builtin_bit_cast(u32, a), __builtin_bit_cast(u32, b), false, false);
  return __builtin_bit_cast(float, (u32)r[0]) + __builtin_bit_cast(float, (u32)r[1]);
}
__device__ __forceinline__ float fold16(float a, float b) {
  auto r = __builtin_amdgcn_permlane16_swap(__builtin_bit_cast(u32, a), __builtin_bit_cast(u32, b), false, false);
  return __builtin_bit_cast(float, (u32)r[0]) + __builtin_bit_cast(float, (u32)r[1]);
}
__device__ __forceinline__ float row_sum(float v) {
  v = dpp_add<0xB1>(v);
  v = dpp_add<0x4E>(v);
  v = dpp_add<0x141>(v);
  return dpp_add<0x140>(v);
}
__device__ __forceinline__ float rdlane(float v, int l) { return __builtin_bit_cast(float, __builtin_amdgcn_readlane(__builtin_bit_cast(int, v), l)); }
__device__ __forceinline__ void dma_w1x6(v4u rsrc, const u32 (&vo)[6], u32 soff, u32 lds_dst) {
  u32 keep, ld;
  asm volatile(
      "s_mov_b32 %0, m0\n\ts_mov_b32 m0, %10\n\ts_add_u32 %1, %10, 0x400\n\t"
      "buffer_load_dwordx4 %2, %8, %9 offen lds\n\ts_mov_b32 m0, %1\n\ts_add_u32 %1, %1, 0x400\n\t"
      "buffer_load_dwordx4 %3, %8, %9 offen lds\n\ts_mov_b32 m0, %1\n\ts_add_u32 %1, %1, 0x400\n\t"
      "buffer_load_dwordx4 %4, %8, %9 offen lds\n\ts_mov_b32 m0, %1\n\ts_add_u32 %1, %1, 0x400\n\t"
      "buffer_load_dwordx4 %5, %8, %9 offen lds\n\ts_mov_b32 m0, %1\n\ts_add_u32 %1, %1, 0x400\n\t"
      "buffer_load_dwordx4 %6, %8, %9 offen lds\n\ts_mov_b32 m0, %1\n\ts_nop 0\n\t"
      "buffer_load_dwordx4 %7, %8, %9 offen lds\n\t"
      "s_mov_b32 m0, %0"
      : "=&s"(keep), "=&s"(ld)
      : "v"(vo[0]), "v"(vo[1]), "v"(vo[2]), "v"(vo[3]), "v"(vo[4]), "v"(vo[5]), "s"(rsrc), "s"(soff), "s"(lds_dst)
      : "scc");
}
#define SC1 16
#define NT 0
#define LD128(off, so) __builtin_amdgcn_raw_buffer_load_b128(rs, (off), (so), SC1)
#define LD64(off, so) __builtin_amdgcn_raw_buffer_load_b64(rs, (off), (so), SC1)
#define LD32(off, so) __builtin_amdgcn_raw_buffer_load_b32(rs, (off), (so), SC1)
#define ST128(v, off, so) __builtin_amdgcn_raw_buffer_store_b128((v), rs, (off) + (so), 0, SC1)
#define ST64(v, off, so) __builtin_amdgcn_raw_buffer_store_b64((v), rs, (off), (so), SC1)
#define ST32(v, off, so) __builtin_amdgcn_raw_buffer_store_b32((v), rs, (off), (so), SC1)
#define DRAIN() asm volatile("s_waitcnt vmcnt(0)" ::: "memory")
#define CFENCE() asm volatile("" ::: "memory")
#define SB() __builtin_amdgcn_sched_barrier(0)
#define BAR() asm volatile("s_waitcnt lgkmcnt(0)\n\ts_barrier" ::: "memory")

template <int IDX> __device__ __forceinline__ void lds_sig(u32 base, u32 one) {
  unsigned long long sv;
  asm volatile("s_mov_b64 %0, exec\n\ts_mov_b64 exec, 1\n\tds_add_u32 %1, %2 offset:%3\n\ts_mov_b64 exec, %0" : "=&s"(sv) : "v"(base), "v"(one), "i"(IDX * 4) : "memory");
}
__device__ __forceinline__ u32 lds_arrive(u32 addr, u32 one) {
  unsigned long long sv;
  u32 t;
  asm volatile("s_mov_b64 %0, exec\n\ts_mov_b64 exec, 1\n\tds_add_rtn_u32 %1, %2, %3\n\ts_mov_b64 exec, %0\n\ts_waitcnt lgkmcnt(0)"
               : "=&s"(sv), "=&v"(t) : "v"(addr), "v"(one) : "memory");
  return __builtin_amdgcn_readfirstlane(t);
}
template <int VAL, int NAP> __device__ __forceinline__ void lds_wait(u32 addr) {
  u32 n, r, t;
  asm volatile(
      "s_mov_b32 %0, 0x200000\n"
      "1:\n\tds_read_b32 %2, %3\n\ts_waitcnt lgkmcnt(0)\n\tv_readfirstlane_b32 %1, %2\n\ts_cmp_ge_u32 %1, %4\n\ts_cbranch_scc1 2f\n\t"
      "s_sleep %5\n\ts_sub_u32 %0, %0, 1\n\ts_cbranch_scc0 1b\n"
      "2:"
      : "=&s"(n), "=&s"(r), "=&v"(t) : "v"(addr), "i"(VAL), "i"(NAP) : "scc", "memory");
}
typedef u32 v16u __attribute__((ext_vector_type(16)));
struct Desc {
  const void* base;
  u32 bytes, flags;
};
struct Params {
  Desc role[3][8];
  const void* rest[8];
};
__device__ __forceinline__ unsigned long long mk64(u32 lo, u32 hi) { return ((unsigned long long)hi << 32) | lo; }
__device__ __forceinline__ unsigned long long mk64u(u32 lo, u32 hi) { return mk64(__builtin_amdgcn_readfirstlane(lo), __builtin_amdgcn_readfirstlane(hi)); }

__global__ void __launch_bounds__(512, 2) mega(Params prm) {
  extern __shared__ __attribute__((aligned(16))) unsigned char smem[];
  int b = blockIdx.x;
  const int tid = threadIdx.x, lane = tid & 63, t8 = tid & 255;
  const int w = __builtin_amdgcn_readfirstlane(tid >> 6);
  STAMP(0);
  typedef const __attribute__((address_space(4))) unsigned char* kptr;
  const kptr kp = (kptr)__builtin_amdgcn_kernarg_segment_ptr();
  v16u kd0;
  v16u kd1;
  u32 code_pf;
  {
    const u32 roff = (w < 4 ? 0 : (w >> 1) - 1) * 128;
    unsigned long long pc;
    u32 clen;
    asm volatile("s_load_dwordx16 s[64:79], %5, %6\n\ts_load_dwordx16 s[80:95], %5, %7\n\ts_getpc_b64 %2\n.Lmega_pc:\n\ts_mov_b32 %4, .Lmega_end-.Lmega_pc"
                 : "={s[64:79]}"(kd0), "={s[80:95]}"(kd1), "=&s"(pc), "+s"(b), "=&s"(clen)
                 : "s"(kp), "s"(roff), "s"(roff + 64));
    const v4u rc = {(u32)pc, (u32)(pc >> 32), clen, 0x00020000u};
    asm volatile("buffer_load_dword %0, %1, %2, 0 offen ; code" : "=&v"(code_pf) : "v"(tid < 8 ? (b >> 3) * 1024 + tid * 128 : 0x70000000), "s"(rc));
    SB();
  }
  const int wl = w & 3;
  const bool mainw = w < 4;
  const int grp = b & 7, mem = b >> 3;
  const int c = lane & 15, g = lane >> 4;
  const int pair = grp * 32 + mem, c0 = 2 * pair;
  const int rp = 2 * grp + (mem >> 4), ct1 = mem & 15;
  const int f0 = 80 * ct1 + 8 * (ct1 < 12 ? ct1 : 12);
  const int noct = ct1 < 12 ? 11 : 10;
  const int rt = 4 * grp + (mem >> 3), ct2 = mem & 7;
  constexpr int NH = 11 - NM;
  const int nk2 = mainw ? NM : w < 7 ? NH : NH - 1;
  const int ksb = (rt & 1) * 22 + (rt & 2) * 5 + (mainw ? NM * wl : 4 * NM + NH * wl);
  const int ks0 = ksb >= 43 ? ksb - 43 : ksb;
  const int co = lane & 7, tq = t8 >> 3;
  const int ta = mem * 64 + 2 * tq;
  const int cb = grp * 64 + 8 * co;
  LAS u32* lcnt = (LAS u32*)(smem + L_CNT);
  if (tid < 24) lcnt[tid] = 0u;
  const int mq = mem >> 2, kq = mem & 3;

  v4u L[8], Le;
  u32 epoch_v;
  {
    const bool ww = (w >> 1) == 2;
    int bM = (mem * 64 + 16 * w) * 1024 + grp * 128, bW = (kq * 128 + 64 * wl) * 6144 + mq * 1024 + grp * 128, bX = (8 * b + 4 * (w - 6)) * 1024;
    int bH = (c0 + w - 6) * 4096;
    asm volatile("" : "+s"(bM), "+s"(bW), "+s"(bX), "+s"(bH));
    bW = mem < (LATEW ? 12 : 24) ? bW : 0x70000000;
    const int rstr = mainw ? 2048 : ww ? 8 * 6144 : 128;
    const int step = ww ? 6144 : 1024;
    const int baseA = mainw ? bM : ww ? bW : bX;
    const int base2 = baseA + (mainw ? 0 : 2 * step);
    const int base4 = w >= 6 ? bH : baseA + (mainw ? 0 : 4 * 6144);
    const int va = (lane >> 3) * rstr + co * 16;
    int o[8];
    o[0] = va + baseA;
    o[1] = va + (baseA + step);
    o[2] = va + base2;
    o[3] = va + (base2 + step);
    o[4] = va + base4;
    o[5] = va + (base4 + step);
    int o6a = lane < 24 ? bM - 3072 + (lane >> 3) * 1024 + co * 16 : 0x70000000, o6b = va + (base4 + 2 * step);
    int o7a = (grp * 512 + lane * 16) | ((lane >> 5) << 30), o7b = va + (base4 + 3 * step);
    const int eb = mainw ? f0 * 2 : ww ? (kq * 128 + 64 * wl) * 2 : ct2 * 128, el = ww ? 64 : w == 0 ? 12 : w == 6 ? 8 : 0;
    const int oe = lane < el ? eb + (ww ? lane >> 3 : lane) * 16 : 0x70000000;
    asm volatile("" : "+v"(o6a), "+v"(o6b), "+v"(o7a), "+v"(o7b));
    o[6] = mainw ? o6a : o6b;
    o[7] = mainw ? o7a : o7b;
    asm volatile(
        "s_waitcnt lgkmcnt(0)\n\ts_barrier\n\t"
        "buffer_load_dwordx4 %0, %11, s[64:67], 0 offen" BNT "\n\tbuffer_load_dwordx4 %1, %12, s[64:67], 0 offen" BNT "\n\t"
        "buffer_load_dwordx4 %2, %13, s[68:71], 0 offen" BNT "\n\tbuffer_load_dwordx4 %3, %14, s[68:71], 0 offen" BNT "\n\t"
        "buffer_load_dwordx4 %6, %17, s[76:79], 0 offen" BNT "\n\tbuffer_load_dwordx4 %7, %18, s[80:83], 0 offen" BNT "\n\t"
        "buffer_load_dwordx4 %8, %19, s[84:87], 0 offen\n\t"
        "buffer_load_dwordx4 %4, %15, s[72:75], 0 offen" BNT "\n\tbuffer_load_dwordx4 %5, %16, s[72:75], 0 offen" BNT "\n\t"
        "buffer_load_dword %9, off, s[88:91], 0"
        : "=&v"(L[0]), "=&v"(L[1]), "=&v"(L[2]), "=&v"(L[3]), "=&v"(L[4]), "=&v"(L[5]), "=&v"(L[6]), "=&v"(L[7]), "=&v"(Le), "=&v"(epoch_v),
          "+{s[80:95]}"(kd1)
        : "v"(o[0]), "v"(o[1]), "v"(o[2]), "v"(o[3]), "v"(o[4]), "v"(o[5]), "v"(o[6]), "v"(o[7]), "v"(oe), "{s[64:79]}"(kd0)
        : "memory");
  }
  typedef u32 v16a __attribute__((ext_vector_type(16), aligned(16)));
  kptr kp2 = kp;
  asm volatile("" : "+s"(kp2));
  const v16u kb = *(const __attribute__((address_space(4))) v16a*)(kp2 + 384);
  GAS unsigned char* ws = (GAS unsigned char*)mk64u(kd1[8], kd1[9]);
  const unsigned long long late_src = mk64u(kd1[0], kd1[1]);
  const GAS u16* ssmD = (const GAS u16*)mk64(kb[4], kb[5]);
  const GAS u16* w1 = (const GAS u16*)mk64u(kd1[12], kd1[13]);
  const GAS u16* w2 = (const GAS u16*)mk64(kb[10], kb[11]);
  GAS u16* out = (GAS u16*)mk64(kb[14], kb[15]);
  const __amdgpu_buffer_rsrc_t rs = __builtin_amdgcn_make_buffer_rsrc((void*)ws, 0, WS_END, 0x00020000);
  SB();

  v2u w2r[NH][8];
#define KS2(i) (ks0 + (i) >= 43 ? (i) - 43 : (i))
  const __amdgpu_buffer_rsrc_t rw2 = __builtin_amdgcn_make_buffer_rsrc((void*)w2, 0, F * D * 2, 0x00020000);
  const int w2v = (8 * g * D + 4 * c) * 2, w2s = (32 * ks0 * D + 64 * ct2) * 2;
#define W2_LOAD(i)                                                                                                   \
  if ((i) < NH - 1 || w < 7) {                                                                                        \
    _Pragma("unroll") for (int jj = 0; jj < 8; ++jj)                                                                 \
        w2r[i][jj] = __builtin_amdgcn_raw_buffer_load_b64(rw2, w2v + (jj & 3) * 1024, w2s + KS2(i) * 32768 + (jj >> 2) * 4096, 0); \
  }
  const v4u rw1 = {(u32)(unsigned long long)w1, (u32)((unsigned long long)w1 >> 32), (u32)(D * F * 2), 0x00020000u};
#define W1_DMA(ks) dma_w1x6(rw1, dvo, (32 * (ks) * F + f0) * 2, (u32)(uintptr_t)(LAS unsigned char*)smem + L_W1 + (ks) * 6144)
  const u32 lca = (u32)(uintptr_t)lcnt;
#define WAIT_LDS(idx, val) lds_wait<(int)(val), 1>(lca + (idx) * 4)
#define WAIT_LDS0(idx, val) lds_wait<(int)(val), 0>(lca + (idx) * 4)
  f32x4* red = (f32x4*)(smem + L_RED);
  const int r5 = lane & 31;
  u32 mk[4];
  const LAS unsigned char *trb, *svp;
  LAS unsigned char* wrb;
#define W1P_SETUP()                                                                                                     \
  {                                                                                                                     \
    const int hh = lane >> 5, rg = (r5 & 3) + 4 * (r5 >> 3);                                                            \
    _Pragma("unroll") for (int i = 0; i < 4; ++i)                                                                       \
        mk[i] = r5 >= 16 ? (r5 < 18 ? 0xffffffffu : 0u)                                                                 \
                         : (hh == ((r5 >> 2) & 1) && (rg >> 1) == i) ? ((rg & 1) ? 0xffff0000u : 0x0000ffffu) : 0u;     \
    wrb = (LAS unsigned char*)smem + L_W1 + w * 12288 + lane * 16;                                                      \
    trb = (LAS unsigned char*)smem + L_W1 + w * 12288 + (8 * hh + (c >> 2)) * 192 + (4 * (c & 1) + 2 * (g & 1) + ((c >> 1) & 1)) * 8; \
    svp = (LAS unsigned char*)smem + (r5 == 17 ? L_SB : L_SA) + (64 * w + 8 * hh) * 2;                                  \
    asm volatile("" : "+v"(mk[0]), "+v"(mk[1]), "+v"(mk[2]), "+v"(mk[3]), "+v"(wrb), "+v"(trb), "+v"(svp));             \
  }
  v4u fin;
#define FIN_LD() fin = LD128((lane >> 1) * FIN_STR + (lane & 1) * 16, WS_FIN + w * 32 * FIN_STR)
#define SIG_LDS(idx) lds_sig<idx>(lca, 1u)
#define ARRIVE(idx) lds_arrive(lca + (idx) * 4, 1u)

  u32 pay = 0, scp[4] = {0, 0, 0, 0};
  ST32(0u, tid == 384 ? 0 : 0x7fff0000, WS_CNT1 + b * 256);
  if (tid < 256 && lane < 28) *(v4u*)(smem + L_U + w * C_UCOPY + 4096 + lane * 16) = v4u{0, 0, 0, 0};
  u32 dw = *(const __attribute__((address_space(4))) u32*)(unsigned long long)(ssmD + c0);
  asm volatile("" : "+s"(dw));
  asm volatile("s_waitcnt vmcnt(4) ; first" : "+v"(L[0]), "+v"(L[1]), "+v"(L[2]), "+v"(L[3]), "+v"(L[6]), "+v"(L[7]), "+v"(Le), "+v"(code_pf)::"memory");
  STAMP(29);
  if (w >= 6) {
      if (tid < 392) *(v4u*)(smem + L_CS + 512 + lane * 16) = Le;
      f32x2 e[4][4];
      float sm[4];
#pragma unroll
      for (int r = 0; r < 4; ++r) {
#pragma unroll
        for (int q = 0; q < 4; ++q) e[r][q] = bf_pair(L[r][q]);
        const f32x2 t = (e[r][0] + e[r][1]) + (e[r][2] + e[r][3]);
        sm[r] = t[0] + t[1];
      }
      const float m = row_sum(fold16(fold32(sm[0], sm[2]), fold32(sm[1], sm[3]))) * (1.f / D);
#pragma unroll
      for (int r = 0; r < 4; ++r) {
        const f32x2 mr = splat(rdlane(m, 16 * r));
        f32x2 t = {0.f, 0.f};
#pragma unroll
        for (int q = 0; q < 4; ++q) {
          const f32x2 dlt = e[r][q] - mr;
          t = fma2(dlt, dlt, t);
        }
        sm[r] = t[0] + t[1];
      }
      const float var = row_sum(fold16(fold32(sm[0], sm[2]), fold32(sm[1], sm[3]))) * (1.f / D);
      pay = pack_bf2(m, __builtin_amdgcn_sqrtf(rbf(rbf(var) + 1e-5f)));
  } else if (w >= 4) {
#pragma unroll
      for (int i = 0; i < 4; ++i) {
        f32x2 sc = bf_pair(Le[i]);
#pragma unroll
        for (int e = 0; e < 2; ++e) sc[e] = sc[e] * __builtin_amdgcn_rcpf(1.f + __expf(-sc[e]));
        scp[i] = pack2(sc);
      }
  } else {
    unsigned char* kp_ = smem + L_RED + 16384 + w * 4096 + 384 + (lane >> 3) * 256 + co * 16;
    *(v4u*)kp_ = L[0];
    *(v4u*)(kp_ + 128) = L[1];
    if (tid < 12) *(v4u*)(smem + L_CS + lane * 16) = Le;
    v4u kr[5], sk[4];
    const v4u vr[2] = {L[2], L[3]};
    {
      unsigned char* sp = smem + L_RED + 16384 + w * 4096;
      if (lane < 24) *(v4u*)(sp + lane * 16) = L[6];
      *(v4u*)(sp + 2560 + lane * 16) = L[7];
#pragma unroll
      for (int i = 0; i < 3; ++i) kr[i] = *(const v4u*)(sp + (lane >> 3) * 256 + co * 16 + i * 128);
#pragma unroll
      for (int i = 0; i < 4; ++i) sk[i] = *(const v4u*)(sp + 2560 + co * 64 + i * 16);
      kr[3] = L[0];
      kr[4] = L[1];
    }
    {
      unsigned char* tp = smem + L_RED + w * 4096;
      const int dq = 7 - (lane >> 3);
#pragma unroll
      for (int j = 0; j < 8; ++j) {
        const u32 s01 = sk[j >> 1][(j & 1) * 2], s23 = sk[j >> 1][(j & 1) * 2 + 1];
        u32 p[4];
#pragma unroll
        for (int i = 0; i < 4; ++i) p[i] = (j & 1) ? perm_hi(kr[i][j >> 1], kr[i + 1][j >> 1]) : perm_lo(kr[i][j >> 1], kr[i + 1][j >> 1]);
        const float ks0v = dot2(p[2], s01, dot2(p[0], s23, 0.f));
        const float ks1v = dot2(p[3], s01, dot2(p[1], s23, 0.f));
        const float v0 = (j & 1) ? bf_hi(vr[0][j >> 1]) : bf_lo(vr[0][j >> 1]);
        const float v1 = (j & 1) ? bf_hi(vr[1][j >> 1]) : bf_lo(vr[1][j >> 1]);
        *(u32*)(tp + co * 512 + (j >> 1) * 128 + (((2 * (j & 1) + (dq >> 2)) ^ co) << 4) + (dq & 3) * 4) = pack2(f32x2{ks1v, ks0v} * f32x2{v1, v0});
      }
    }
  }
  asm volatile("s_waitcnt vmcnt(0)" : "+v"(L[4]), "+v"(L[5]), "+v"(epoch_v)::"memory");
  const u32 tag = __builtin_amdgcn_readfirstlane(epoch_v) + 1u;

  if (mainw) {
    {
      unsigned char* tp = smem + L_RED + w * 4096;
#pragma unroll
      for (int q = 0; q < 4; ++q)
        *(v2u*)(tp + co * 512 + q * 128 + (((4 + (lane >> 4)) ^ co) << 4) + ((lane >> 3) & 1) * 8) = v2u{L[4][q], L[5][q]};
      STAMP(1);
#pragma unroll
      for (int i = 0; i < 4; ++i) {
        const int q = lane + 64 * i;
        const v4u o = *(const v4u*)(tp + ((q ^ (q >> 5)) << 4));
        ST128(o, lane * 16 + 1024 * i, WS_KVR + (grp * 128 + 127 - 4 * mem - w) * KV_STR);
      }
    }
    SIG_LDS(12);
    STAMP(2);
    DRAIN();
    if (ARRIVE(6) == 3u) ST32(tag, lane == 0 ? 0 : 0x7fff0000, WS_FLAG0 + (grp * 32 + mem) * 128);
    STAMP(3);
    const int ch = w >> 1, hf = w & 1;
    const LAS u32* hb4[4];
#pragma unroll
    for (int i = 0; i < 4; ++i) {
      hb4[i] = (const LAS u32*)(smem + L_H + ch * C_HCH) + (c + 4 * g + 1 + 64 * hf) + 256 * i;
      asm volatile("" : "+v"(hb4[i]));
    }
    const LAS unsigned char* ub = (const LAS unsigned char*)smem + L_U + ch * C_UCH + (c & 1) * C_UCOPY + 2 * (2016 - 1792 - 32 * (c >> 1) + 8 * g) + 256 * hf;
    asm volatile("" : "+v"(ub));
    v4u hfr[2][8], ufr[2][8];
    WAIT_LDS(12, 4u);
    v4u dfr;
    {
      const u32 dvv = hf ? 0u : (ch ? dw >> 16 : dw & 0xffffu);
      const int jd = 15 - c - 4 * g;
#pragma unroll
      for (int i = 0; i < 4; ++i) dfr[i] = jd == i ? dvv : 0u;
      asm volatile("" : "+v"(dfr));
    }
    const bool isx = co >= 4;
    const int fso = WS_KVR + (grp * 128 + 32 * w) * KV_STR + mem * 128;
    int fvo = (lane >> 3) * 4 * KV_STR + co * 16, fvn = (w < 3 && lane >= 56 && !isx) ? co * 16 : 0x7fff0000;
    LAS unsigned char* ud = (LAS unsigned char*)smem + (isx ? L_RED + (127 - 32 * w - 4 * (lane >> 3)) * 64 + (co - 4) * 16
                                                           : L_U + C_UCOPY + (co >> 1) * C_UCH + w * 1024 + (lane >> 3) * 128 + (co & 1) * 16);
    asm volatile("" : "+v"(fvo), "+v"(fvn), "+v"(ud));
    {
      int pm = 31 - 8 * w - (lane < 9 ? lane : 0);
      pm = pm < 0 ? 0 : pm;
      __builtin_amdgcn_s_sleep(MSLEEP0);
#pragma unroll 1
      for (u32 sp = 0; sp < SPIN_CAP; ++sp) {
        CFENCE();
        const u32 f = LD32(pm * 128, WS_FLAG0 + grp * 4096);
        if (__all(f == tag)) break;
        __builtin_amdgcn_s_sleep(MSLEEP);
      }
      CFENCE();
    }
    STAMP(4);
    {
      v4u uv[4];
#pragma unroll
      for (int i = 0; i < 4; ++i) uv[i] = LD128(fvo, fso + i * KV_STR);
      const u32 nx4 = LD32(fvn, fso + 32 * KV_STR);
      SIG_LDS(2);
      WAIT_LDS(15, 2u);
#pragma unroll
      for (int s = 0; s < 8; ++s)
#pragma unroll
        for (int jj = 0; jj < 4; ++jj) hfr[0][s][jj] = hb4[s >> 1][128 * (s & 1) + jj];
#pragma unroll
      for (int s = 0; s < 8; ++s) asm volatile("" : "+v"(hfr[0][s]));
      u32 n0 = __builtin_amdgcn_ds_bpermute(((lane + 8) & 63) * 4, uv[0][0]);
#pragma unroll
      for (int i = 0; i < 4; ++i) *(LAS v4u*)(ud + (isx ? -64 : 32) * i) = uv[i];
      n0 = lane >= 56 ? nx4 : n0;
      if (!isx) {
#pragma unroll
        for (int i = 0; i < 4; ++i) {
          const u32 give = (co & 1) ? uv[i][0] : (i < 3 ? uv[i < 3 ? i + 1 : 3][0] : n0);
          const u32 un = __builtin_amdgcn_update_dpp(0u, give, 0xB1, 0xf, 0xf, true);
          v4u sh;
          sh[0] = __builtin_amdgcn_alignbit(uv[i][1], uv[i][0], 16);
          sh[1] = __builtin_amdgcn_alignbit(uv[i][2], uv[i][1], 16);
          sh[2] = __builtin_amdgcn_alignbit(uv[i][3], uv[i][2], 16);
          sh[3] = __builtin_amdgcn_alignbit(un, uv[i][3], 16);
          *(LAS v4u*)(ud + 32 * i - C_UCOPY) = sh;
        }
      }
      SIG_LDS(0);
    }
    WAIT_LDS0(0, 4u);
    STAMP(5);
    __builtin_amdgcn_s_setprio(2);
    f32x4 acc[8];
#pragma unroll
    for (int n = 0; n < 8; ++n) acc[n] = f32x4{0.f, 0.f, 0.f, 0.f};
#define CONV_LD(buf, rr, s)                                                                                            \
  {                                                                                                                    \
    ufr[buf][s] = *(const LAS v4u*)(ub + 64 * (rr) + 512 * (7 - (s)));                                                 \
    _Pragma("unroll") for (int jj = 0; jj < 4; ++jj) hfr[buf][s][jj] = hb4[(s) >> 1][16 * (rr) + 128 * ((s) & 1) + jj]; \
  }
#pragma unroll
    for (int s = 0; s < 8; ++s) ufr[0][s] = *(const LAS v4u*)(ub + 512 * (7 - s));
    SB();
#pragma unroll
    for (int rr = 0; rr < 4; ++rr) {
#pragma unroll
      for (int n = 0; n < 8; ++n) {
        if (rr < 3 && n >= 4) {
          CONV_LD((rr + 1) & 1, rr + 1, 2 * (n - 4));
          CONV_LD((rr + 1) & 1, rr + 1, 2 * (n - 4) + 1);
        }
        if (rr == 3 && n == 0 && w == 0) WAIT_LDS0(16, 4u);
        if (rr == 3 && n > 0) red[(w * 8 + n - 1) * 64 + lane] = acc[n - 1];
        if (rr == 0) acc[n] = mfma16(ufr[0][n], dfr, acc[n]);
#pragma unroll
        for (int s = 0; s <= n; ++s) acc[n] = mfma16(ufr[rr & 1][n - s], hfr[rr & 1][s], acc[n]);
#pragma unroll
        for (int s = 0; s <= n; ++s) {
          __builtin_amdgcn_sched_group_barrier(0x8, 1, 0);
          __builtin_amdgcn_sched_group_barrier(0x80, 1, 0);
        }
        SB();
      }
    }
    red[(w * 8 + 7) * 64 + lane] = acc[7];
    __builtin_amdgcn_s_setprio(0);
    SIG_LDS(4);
    STAMP(6);
    FIN_LD();
    W1P_SETUP();
    WAIT_LDS(18, 4u);
    asm volatile("; main" : "+v"(fin));
  } else {
   if (w < 6) {
    if (LATEW && mem >= 12 && mem < 24) {
      const __amdgpu_buffer_rsrc_t rm = __builtin_amdgcn_make_buffer_rsrc((void*)late_src, 0, D * 3072 * 2, 0x00020000);
      WAIT_LDS(12, 4u);
#pragma unroll
      for (int i = 0; i < 8; ++i)
        L[i] = __builtin_amdgcn_raw_buffer_load_b128(rm, (lane >> 3) * (8 * 6144) + co * 16, (kq * 128 + 64 * wl + i) * 6144 + mq * 1024 + grp * 128, NT);
    }
    float a[8];
#pragma unroll
    for (int j = 0; j < 8; ++j) {
      a[j] = 0.f;
#pragma unroll
      for (int i = 0; i < 4; ++i)
        a[j] = dot2((j & 1) ? perm_hi(L[2 * i + 1][j >> 1], L[2 * i][j >> 1]) : perm_lo(L[2 * i + 1][j >> 1], L[2 * i][j >> 1]), scp[i], a[j]);
    }
    float r[2];
#pragma unroll
    for (int j = 0; j < 2; ++j) r[j] = dpp_add<0x128>(fold16(fold32(a[j], a[4 + j]), fold32(a[2 + j], a[6 + j])));
    const int src = ((lane & 3) << 4) + ((lane >> 2) & 7);
    const float r0 = __shfl(r[0], src), r1 = __shfl(r[1], src);
    ST128((v4u{__builtin_bit_cast(u32, r0), tag, __builtin_bit_cast(u32, r1), tag}), (mem < 24 && lane < 32) ? lane * 16 : 0x7fff0000,
          WS_MODP + ((kq * 2 + wl) * 3072 + mq * 512 + grp * 64) * 8);
    STAMP(2);
   } else {
      const int e = w - 6;
      const u32 p1 = __builtin_amdgcn_readlane(pay, 16), p2 = __builtin_amdgcn_readlane(pay, 32), p3 = __builtin_amdgcn_readlane(pay, 48);
      ST128((v4u{lane ? p2 : pay, tag, lane ? p3 : p1, tag}), lane < 2 ? lane * 16 : 0x7fff0000, WS_ST1 + (8 * b + 4 * e) * 8);
      STAMP(2);
      unsigned char* hd = smem + L_H + e * C_HCH;
      if (lane < 4) *(v4u*)(hd + lane * 16) = v4u{0, 0, 0, 0};
#pragma unroll
      for (int i = 0; i < 4; ++i) *(v4u*)(hd + 64 + lane * 16 + i * 1024) = L[4 + i];
      SIG_LDS(15);
   }
    u32 dvo[6];
#pragma unroll
    for (int n = 0; n < 6; ++n) {
      const int i = 64 * n + lane, row = i / 12, slot = i % 12;
      dvo[n] = row * (F * 2) + (slot < noct ? slot : 0) * 16;
    }
    const int tb = 512 * wl + 64 * g + 2 * c;
    const int tb4 = tb * 4, tb8 = tb * 8;
    v4u stv[4];
    v2u mp[2];
    const int mvo = w == 4 ? ((g * 2 * 3072) + ((c < 12 ? c : 0) >> 1) * 512 + (c & 1)) * 8 : 0x7fff0000;
    {
      const v4u rws = {(u32)(unsigned long long)ws, (u32)((unsigned long long)ws >> 32), (u32)WS_END, 0x00020000u};
      int so = tb8 + WS_ST1, mo = mvo;
      asm volatile("" : "+v"(so), "+v"(mo), "+v"(dvo[0]), "+v"(dvo[1]), "+v"(dvo[2]), "+v"(dvo[3]), "+v"(dvo[4]), "+v"(dvo[5]));
      STAMP(1);
      WAIT_LDS(2, 4u);
      asm volatile(
          "buffer_load_dwordx2 %4, %7, %8, %9 offen sc1 ; ops6\n\tbuffer_load_dwordx2 %5, %7, %8, %10 offen sc1\n\t"
          "buffer_load_dwordx4 %0, %6, %8, 0 offen sc1\n\tbuffer_load_dwordx4 %1, %6, %8, 0 offen offset:256 sc1\n\t"
          "buffer_load_dwordx4 %2, %6, %8, 0 offen offset:2048 sc1\n\tbuffer_load_dwordx4 %3, %6, %8, 0 offen offset:2304 sc1"
          : "=&v"(stv[0]), "=&v"(stv[1]), "=&v"(stv[2]), "=&v"(stv[3]), "=&v"(mp[0]), "=&v"(mp[1])
          : "v"(so), "v"(mo), "s"(rws), "s"(WS_MODP + c0 * 8), "s"(WS_MODP + (3072 + c0) * 8));
    }
    __builtin_amdgcn_s_sleep(DSLEEP);
    if (w == 4) {
      DRAIN();
    } else {
      W1_DMA(2 * wl);
      W1_DMA(2 * wl + 1);
    }
    asm volatile("s_waitcnt vmcnt(12) ; operands" : "+v"(stv[0]), "+v"(stv[1]), "+v"(stv[2]), "+v"(stv[3]), "+v"(mp[0]), "+v"(mp[1])::"memory");
    STAMP(4);
    float* mds = (float*)(smem + L_CS + 640);
    if (w == 4) {
      if (!__all(((mp[0][1] ^ tag) | (mp[1][1] ^ tag)) == 0)) {
#pragma unroll 1
        for (u32 sp = 0; sp < SPIN_CAP; ++sp) {
          CFENCE();
#pragma unroll
          for (int i = 0; i < 2; ++i) mp[i] = LD64(mvo, WS_MODP + (i * 3072 + c0) * 8);
          if (__all(((mp[0][1] ^ tag) | (mp[1][1] ^ tag)) == 0)) break;
          __builtin_amdgcn_s_sleep(PSLEEP);
        }
      }
      float md = __builtin_bit_cast(float, mp[0][0]) + __builtin_bit_cast(float, mp[1][0]);
      md = fold16(md, md);
      md = rbf(fold32(md, md));
      if (lane < 16) mds[lane] = md;
      SIG_LDS(17);
      const float sbv = lane ? rdlane(md, 7) : rdlane(md, 6), cbv = lane ? rdlane(md, 9) : rdlane(md, 8), gbv = lane ? rdlane(md, 11) : rdlane(md, 10);
      ST128((v4u{pack_bf2(1.f + cbv, sbv), tag, __builtin_bit_cast(u32, gbv), tag}), lane < 2 ? lane * 16 : 0x7fff0000, WS_FIN + pair * FIN_STR);
    }
#define ST_BAD() ((stv[0][1] ^ tag) | (stv[0][3] ^ tag) | (stv[1][1] ^ tag) | (stv[1][3] ^ tag) | (stv[2][1] ^ tag) | (stv[2][3] ^ tag) | \
                  (stv[3][1] ^ tag) | (stv[3][3] ^ tag))
    if (!__all(ST_BAD() == 0)) {
#pragma unroll 1
      for (u32 sp = 0; sp < SPIN_CAP; ++sp) {
        CFENCE();
#pragma unroll
        for (int i = 0; i < 4; ++i) stv[i] = LD128(tb8 + (256 * (i >> 1) + 32 * (i & 1)) * 8, WS_ST1);
        if (__all(ST_BAD() == 0)) break;
        __builtin_amdgcn_s_sleep(PSLEEP);
      }
    }
    WAIT_LDS(0, 4u);
    v2u xv[4];
#pragma unroll
    for (int i = 0; i < 4; ++i) xv[i] = *(const v2u*)(smem + L_RED + tb4 + (256 * (i >> 1) + 32 * (i & 1)) * 4);
    SIG_LDS(16);
    WAIT_LDS(17, 1u);
    const f32x2 sa = *(const f32x2*)mds, cr = *(const f32x2*)(mds + 2), ga = *(const f32x2*)(mds + 4);
    const f32x2 gca = ga * bf_pair(pack2(cr + splat(1.f))), gsa = ga * sa;
    STAMP(5);
    f32x2 P[4][2], Q[4][2];
#pragma unroll
    for (int i = 0; i < 4; ++i) {
#pragma unroll
      for (int r = 0; r < 2; ++r) {
        Q[i][r] = bf_pair(xv[i][r]);
        const u32 sw = stv[i][2 * r];
        P[i][r] = fma2(Q[i][r] - splat(bf_lo(sw)), splat(__builtin_amdgcn_rcpf(bf_hi(sw))) * gca, gsa);
        asm volatile("" : "+v"(P[i][r]), "+v"(Q[i][r]));
      }
      if (i == 0) W1_DMA(w == 4 ? 0 : w == 7 ? 1 : 2 * w);
      if (i == 1 && (w == 4 || w == 7)) W1_DMA(2 * w);
      if (i == 2) W1_DMA(2 * w + 1);
    }
    asm volatile("s_waitcnt vmcnt(12)" ::: "memory");
    FIN_LD();
    STAMP(6);
    WAIT_LDS(4, 4u);
    __builtin_amdgcn_s_setprio(3);
#pragma unroll
    for (int i = 0; i < 2; ++i) {
      const int n = 2 * wl + i;
      const f32x4 y0 = red[(0 * 8 + n) * 64 + lane] + red[(1 * 8 + n) * 64 + lane];
      const f32x4 y1 = red[(2 * 8 + n) * 64 + lane] + red[(3 * 8 + n) * 64 + lane];
#pragma unroll
      for (int h = 0; h < 2; ++h) {
        v2u o;
#pragma unroll
        for (int r = 0; r < 2; ++r)
          o[r] = pack_bf2(__builtin_fmaf(P[2 * i + h][r][0], y0[2 * h + r], Q[2 * i + h][r][0]),
                          __builtin_fmaf(P[2 * i + h][r][1], y1[2 * h + r], Q[2 * i + h][r][1]));
        ST64(o, tb4 + (256 * i + 32 * h) * 4, WS_X1P + pair * XP_STR);
      }
    }
    SIG_LDS(18);
    STAMP(7);
    W1P_SETUP();
    asm volatile("" : "+v"(fin));
    DRAIN();
    if (ARRIVE(1) == 3u && lane < 32)
      __hip_atomic_fetch_add((GAS u32*)(ws + WS_CNT1 + (lane * 8 + grp) * 256), 1u, __ATOMIC_RELAXED, __HIP_MEMORY_SCOPE_AGENT);
    __builtin_amdgcn_s_setprio(0);
    STAMP(8);
  }

  {
    float* csp = (float*)(smem + L_CSP);
    if (!__all(fin[1] == tag && fin[3] == tag)) {
#pragma unroll 1
      for (u32 sp = 0; sp < SPIN_CAP; ++sp) {
        __builtin_amdgcn_s_sleep(PSLEEP);
        CFENCE();
        FIN_LD();
        if (__all(fin[1] == tag && fin[3] == tag)) break;
      }
    }
    *(u16*)(smem + L_SA + tid * 2) = (u16)fin[0];
    *(u16*)(smem + L_SB + tid * 2) = (u16)(fin[0] >> 16);
    *(u16*)(smem + L_GB + tid * 2) = (u16)(fin[2] >> 16);
    STAMP(16);
    f32x2 cs2[3] = {{0.f, 0.f}, {0.f, 0.f}, {0.f, 0.f}};
    v4u raw[4][3], Ad[4];
#pragma unroll
    for (int q = 0; q < 4; ++q) {
      const v4u sv = *(const LAS v4u*)(svp + q * 32);
      Ad[q] = v4u{sv[0] & mk[0], sv[1] & mk[1], sv[2] & mk[2], sv[3] & mk[3]};
#pragma unroll
      for (int j = 0; j < 3; ++j) {
        const LAS unsigned char* p = trb + q * 3072 + j * 64;
        const v2u lo = __builtin_bit_cast(v2u, __builtin_amdgcn_ds_read_tr16_b64_v4i16((LAS v4s*)p));
        const v2u hi = __builtin_bit_cast(v2u, __builtin_amdgcn_ds_read_tr16_b64_v4i16((LAS v4s*)(p + 4 * 192)));
        raw[q][j] = v4u{lo[0], lo[1], hi[0], hi[1]};
      }
    }
    CFENCE();
    f32x16 z16;
#pragma unroll
    for (int r = 0; r < 16; ++r) z16[r] = 0.f;
    f32x16 d[12];
    v4u o[12];
    d[0] = mfma32(Ad[0], raw[0][0], z16);
    d[1] = mfma32(Ad[0], raw[0][1], z16);
    SB();
#pragma unroll
    for (int n = 0; n < 12; ++n) {
      if (n + 2 < 12) d[n + 2] = mfma32(Ad[(n + 2) / 3], raw[(n + 2) / 3][(n + 2) % 3], z16);
      SB();
      cs2[n % 3] += f32x2{d[n][8], d[n][9]};
      o[n] = v4u{pack_bf2(d[n][0], d[n][1]), pack_bf2(d[n][2], d[n][3]), pack_bf2(d[n][4], d[n][5]), pack_bf2(d[n][6], d[n][7])};
      if (n % 3 == 2 && r5 == 20) o[n] = v4u{0x3f803f80u, 0x3f803f80u, 0x3f803f80u, 0x3f803f80u};
      *(LAS v4u*)(wrb + n * 1024) = o[n];
      if (n >= 3) asm volatile("" ::"v"(o[n - 3]));
      asm volatile("" : "+v"(cs2[n % 3]));
      if (mainw && (n & 1) == 0 && n / 2 < NM) W2_LOAD(n / 2);
      SB();
    }
    if (lane < 32) {
      const int fc = (r5 & 3) + 4 * (r5 >> 3) + 16 * ((r5 >> 2) & 1);
#pragma unroll
      for (int j = 0; j < 3; ++j) {
        *(f32x2*)(csp + w * 192 + 2 * (32 * j + fc)) = cs2[j];
      }
    }
    STAMP(17);
    SB();
  }
  STAMP(9);
  if (w == 4) {
#pragma unroll 1
    for (u32 sp = 0; sp < SPIN_CAP; ++sp) {
      CFENCE();
      const u32 f = LD32((lane & 7) * 256, WS_CNT1 + (b & 31) * 2048);
      if (__all(f == 32u)) break;
    }
    if (b == 0 && lane == 0) ST32(tag, 0, WS_EPOCH);
  }
  BAR();
  STAMP(10);

  const int qe = (w ^ 4) >> 1, he = w & 1, jp = lane >> 5, ge = 2 * he + ((lane >> 4) & 1);
  const int te = 64 * rt + 32 * (qe >> 1) + 4 * (c & 7) + (c >> 3) + 2 * (qe & 1);
  u32 x1v[4], r2v;
#define FINAL_LD()                                                                           \
  r2v = LD32(c * 4, WS_RS2 + (64 * rt + 16 * qe) * 4);                                       \
  _Pragma("unroll") for (int i = 0; i < 4; ++i) {                                            \
    x1v[i] = LD32((8 * ge + jp) * XP_STR + te * 4, WS_X1P + (32 * ct2 + 2 * i) * XP_STR);    \
  }
  if (mainw) {
    __builtin_amdgcn_s_setprio(2);
    const int t0 = 128 * rp + 32 * w;
    v4u xl[32];
    const int xvo = (lane >> 3) * XP_STR + 16 * (lane & 7), xso = WS_X1P + t0 * 4;
#define X_LD(kk) xl[kk] = LD128(xvo, xso + 8 * (kk) * XP_STR)
    u32 xt0, xt2;
#define X_FRAG_A(kk, A, B, C, E)                                                                                     \
  asm volatile("v_permlane16_swap_b32 v" #A ", v" #C "\n\tv_permlane16_swap_b32 v" #B ", v" #E "\n\tv_mov_b32 %1, v" #A \
               "\n\tv_mov_b32 %2, v" #C                                                                               \
               : "+{v[" #A ":" #E "]}"(xl[kk]), "=&v"(xt0), "=&v"(xt2));
#define X_FRAG_B(kk, A, B, C, E)                                                                                     \
  asm volatile("v_mov_b32_dpp v" #A ", v" #B " row_ror:8 row_mask:0xf bank_mask:0xc\n\t"                              \
               "v_mov_b32_dpp v" #C ", v" #E " row_ror:8 row_mask:0xf bank_mask:0xc\n\t"                              \
               "v_mov_b32_dpp v" #B ", %1 row_ror:8 row_mask:0xf bank_mask:0x3\n\t"                                   \
               "v_mov_b32_dpp v" #E ", %2 row_ror:8 row_mask:0xf bank_mask:0x3"                                       \
               : "+{v[" #A ":" #E "]}"(xl[kk]) : "v"(xt0), "v"(xt2));
#pragma unroll
    for (int kk = 0; kk < PF1; ++kk) X_LD(kk);
    f32x16 acc[3];
#pragma unroll
    for (int j = 0; j < 3; ++j)
#pragma unroll
      for (int r = 0; r < 16; ++r) acc[j][r] = 0.f;
    f32x4 sq[2] = {{0.f, 0.f, 0.f, 0.f}, {0.f, 0.f, 0.f, 0.f}};
    const LAS unsigned char* wfl[2] = {(const LAS unsigned char*)smem + L_W1 + lane * 16, (const LAS unsigned char*)smem + L_W1 + 49152 + lane * 16};
    asm volatile("" : "+v"(wfl[0]), "+v"(wfl[1]));
    v4u wf[2][3];
#define W_LD(kk) \
  { _Pragma("unroll") for (int j = 0; j < 3; ++j) wf[(kk) & 1][j] = *(const LAS v4u*)(wfl[(kk) >> 4] + (((kk) & 15) * 3 + j) * 1024); }
    W_LD(0);
    X_FRAG_A(0, 200, 201, 202, 203);
    X_FRAG_B(0, 200, 201, 202, 203);
    asm volatile("s_nop 1");
    SB();
#define G1_STEP(kk, A, B, C, E)                                                                 \
  {                                                                                             \
    acc[0] = mfma32(wf[(kk) & 1][0], xl[kk], acc[0]);                                           \
    SB();                                                                                       \
    if ((kk) + 1 < 32) W_LD((kk) + 1);                                                          \
    if ((kk) + PF1 < 32) X_LD((kk) + PF1 < 32 ? (kk) + PF1 : 31);                               \
    SB();                                                                                       \
    acc[1] = mfma32(wf[(kk) & 1][1], xl[kk], acc[1]);                                           \
    SB();                                                                                       \
    if ((kk) + 1 < 32) { X_FRAG_A((kk) + 1 < 32 ? (kk) + 1 : 31, A, B, C, E); }                 \
    SB();                                                                                       \
    acc[2] = mfma32(wf[(kk) & 1][2], xl[kk], acc[2]);                                           \
    SB();                                                                                       \
    if ((kk) + 1 < 32) { X_FRAG_B((kk) + 1 < 32 ? (kk) + 1 : 31, A, B, C, E); }                 \
    SB();                                                                                       \
    sq[0] = mfma_sq(xl[kk][0], xl[kk][1], sq[0]);                                               \
    sq[1] = mfma_sq(xl[kk][2], xl[kk][3], sq[1]);                                               \
    asm volatile("" : "+v"(sq[0]), "+v"(sq[1]));                                                \
    if ((kk) == KSIG) ((volatile LAS u32*)lcnt)[8 + w] = 1u;                                    \
    SB();                                                                                       \
    if ((kk) == 0) STAMP(18);                                                                   \
    if ((kk) == 15) STAMP(19);                                                                  \
  }
    G1_STEP(0, 204, 205, 206, 207)
    G1_STEP(1, 208, 209, 210, 211)
    G1_STEP(2, 212, 213, 214, 215)
    G1_STEP(3, 216, 217, 218, 219)
    G1_STEP(4, 220, 221, 222, 223)
    G1_STEP(5, 224, 225, 226, 227)
    G1_STEP(6, 228, 229, 230, 231)
    G1_STEP(7, 232, 233, 234, 235)
    G1_STEP(8, 236, 237, 238, 239)
    G1_STEP(9, 240, 241, 242, 243)
    G1_STEP(10, 244, 245, 246, 247)
    G1_STEP(11, 200, 201, 202, 203)
    G1_STEP(12, 204, 205, 206, 207)
    G1_STEP(13, 208, 209, 210, 211)
    G1_STEP(14, 212, 213, 214, 215)
    G1_STEP(15, 216, 217, 218, 219)
    G1_STEP(16, 220, 221, 222, 223)
    G1_STEP(17, 224, 225, 226, 227)
    G1_STEP(18, 228, 229, 230, 231)
    G1_STEP(19, 232, 233, 234, 235)
    G1_STEP(20, 236, 237, 238, 239)
    G1_STEP(21, 240, 241, 242, 243)
    G1_STEP(22, 244, 245, 246, 247)
    G1_STEP(23, 200, 201, 202, 203)
    G1_STEP(24, 204, 205, 206, 207)
    G1_STEP(25, 208, 209, 210, 211)
    G1_STEP(26, 212, 213, 214, 215)
    G1_STEP(27, 216, 217, 218, 219)
    WAIT_LDS(3, 2u);
    v4u cf[3];
#pragma unroll
    for (int j = 0; j < 3; ++j) cf[j] = *(const v4u*)(smem + L_CSF + j * 1024 + lane * 16);
    SB();
    G1_STEP(28, 220, 221, 222, 223)
    G1_STEP(29, 224, 225, 226, 227)
    G1_STEP(30, 228, 229, 230, 231)
    G1_STEP(31, 232, 233, 234, 235)
    STAMP(11);
    const f32x4 sv4 = sq[0] + sq[1];
    float sum = acc[2][8], ssq = (lane & 2) ? ((lane & 1) ? sv4[3] : sv4[2]) : ((lane & 1) ? sv4[1] : sv4[0]);
    {
      const auto sw = __builtin_amdgcn_permlane32_swap(__builtin_bit_cast(u32, sum), __builtin_bit_cast(u32, sum), false, false);
      sum = __builtin_bit_cast(float, (u32)sw[1]);
      ssq = fold32(ssq, ssq);
    }
    const float m = sum * (1.f / D);
    const float var = fmaxf(__builtin_fmaf(-m, m, ssq * (1.f / D)), 0.f);
    const float den = __builtin_amdgcn_sqrtf(rbf(rbf(var) + 1e-5f));
    const v4u cb4 = {pack_bf2(-m, -m), pack_bf2(-m, den), pack_bf2(den, den), 0u};
#pragma unroll
    for (int j = 0; j < 3; ++j) acc[j] = mfma32(cf[j], cb4, acc[j]);
    {
      const float rsd = __builtin_amdgcn_rcpf(bf_lo(cb4[2]));
      ST32(__builtin_bit_cast(u32, rsd * rsd), (ct1 == 15 && lane < 32) ? lane * 4 : 0x7fff0000, WS_RS2 + t0 * 4);
    }
#pragma unroll
    for (int o8 = 0; o8 < 6; ++o8) {
      const int j = o8 >> 1, r0 = 8 * (o8 & 1);
      v4u o;
#pragma unroll
      for (int q = 0; q < 4; ++q) {
        const f32x2 a = __builtin_elementwise_max(f32x2{acc[j][r0 + 2 * q], acc[j][r0 + 2 * q + 1]}, f32x2{0.f, 0.f});
        o[q] = pack2(a * a);
      }
      const int slot = 4 * j + (o8 & 1);
      ST128(o, (j < 2 || slot + 2 * jp < noct) ? 2 * jp * HO_STR + (lane & 31) * 16 : 0x7fff0000, WS_HO + ((f0 >> 3) + slot) * HO_STR + t0 * 16);
    }
    STAMP(22);
    DRAIN();
    STAMP(23);
    if (ARRIVE(13 + (w >> 1)) == 1u) ST32(tag, lane == 0 ? 0 : 0x7fff0000, WS_FLAG2 + ((2 * rp + (w >> 1)) * 16 + ct1) * 128);
    __builtin_amdgcn_s_setprio(0);
  } else {
    if (w >= 6) {
      const float* csp = (const float*)(smem + L_CSP);
      const int i = tid - 384;
      if (i < 96) {
        float uv = 0.f, sv = bf2f(((const u16*)(smem + L_CS))[i]);
#pragma unroll
        for (int q = 0; q < 8; ++q) {
          const f32x2 t = *(const f32x2*)(csp + 192 * q + 2 * i);
          uv += t[0];
          sv += t[1];
        }
        const float u0 = rbf(uv), u1 = rbf(uv - u0), s0 = rbf(sv), s1 = rbf(sv - s0);
        const int r = (i & 3) + 8 * ((i >> 2) & 3) + 4 * ((i >> 4) & 1);
        unsigned char* dst = smem + L_CSF + (i >> 5) * 1024 + r * 16;
        *(v4u*)dst = v4u{pack_bf2(u0, u1), pack_bf2(uv - u0 - u1, s0), pack_bf2(s1, sv - s0 - s1), 0u};
        *(v4u*)(dst + 512) = v4u{0u, 0u, 0u, 0u};
      }
      SIG_LDS(3);
    }
#pragma unroll 1
    for (u32 sp = 0; sp < SPIN_CAP; ++sp) {
      CFENCE();
      const v4u f = *(volatile LAS v4u*)(lcnt + 8);
      if ((f[0] & f[1] & f[2] & f[3]) != 0u) break;
      __builtin_amdgcn_s_sleep(2);
    }
#pragma unroll
    for (int i = 0; i < NH; ++i) W2_LOAD(i);
    STAMP(11);
  }
  STAMP(12);
  SB();
  v4u wf2[NH][4];
#pragma unroll
  for (int i = 0; i < NH; ++i)
    if (i < NM || !mainw) {
#pragma unroll
      for (int j = 0; j < 4; ++j) {
#pragma unroll
        for (int p = 0; p < 4; ++p)
          wf2[i][j][p] = (j & 1) ? perm_hi(w2r[i][2 * p + 1][j >> 1], w2r[i][2 * p][j >> 1]) : perm_lo(w2r[i][2 * p + 1][j >> 1], w2r[i][2 * p][j >> 1]);
        asm volatile("" : "+v"(wf2[i][j]));
      }
    }
  SB();
  STAMP(20);
  if (w == 7) {
    WAIT_LDS(13, 2u);
#pragma unroll 1
    for (u32 sp = 0; sp < SPIN_CAP; ++sp) {
      CFENCE();
      const u32 f = LD32(c * 128, WS_FLAG2 + rt * 2048);
      if (__all(f == tag)) break;
    }
  }
  BAR();
  STAMP(13);

  {
    if (G2PRIO && !mainw) __builtin_amdgcn_s_setprio(1);
    const int hvo = g * HO_STR + c * 16, hso = WS_HO + 4 * ks0 * HO_STR + 64 * rt * 16;
    const int hvl = w < 7 ? hvo : 0x7fff0000;
    f32x4* red2 = (f32x4*)smem;
#define H_LD(N, n) \
  { _Pragma("unroll") for (int q = 0; q < 2; ++q) hf[n][q] = LD128(((n) % N < NH - 1 ? hvo : hvl) + (2 * ((n) / N) + q) * 256, hso + KS2((n) % N) * 4 * HO_STR); }
#define G2_BODY(N, PF)                                                                                       \
  {                                                                                                          \
    v4u hf[2 * N][2];                                                                                        \
    _Pragma("unroll") for (int n = 0; n < PF; ++n) H_LD(N, n);                                               \
    SB();                                                                                                    \
    _Pragma("unroll") for (int ps = 0; ps < 2; ++ps) {                                                       \
      f32x4 acc[4][2];                                                                                       \
      _Pragma("unroll") for (int j = 0; j < 4; ++j) _Pragma("unroll") for (int q = 0; q < 2; ++q) acc[j][q] = f32x4{0.f, 0.f, 0.f, 0.f}; \
      _Pragma("unroll") for (int i = 0; i < N; ++i) {                                                        \
        if (N * ps + i + PF < 2 * N) H_LD(N, N * ps + i + PF);                                               \
        if (N * ps + i + PF == 2 * N) { FINAL_LD(); }                                                        \
        if (i < NH - 1 || w < 7) {                                                                           \
          _Pragma("unroll") for (int q = 0; q < 2; ++q) _Pragma("unroll") for (int j = 0; j < 4; ++j)        \
              acc[j][q] = mfma16(wf2[i][j], hf[N * ps + i][q], acc[j][q]);                                   \
        }                                                                                                    \
        SB();                                                                                                \
        if (ps == 0 && i == 0) STAMP(24);                                                                    \
      }                                                                                                      \
      if (ps == 1) STAMP(26);                                                                                \
      _Pragma("unroll") for (int q = 0; q < 2; ++q) _Pragma("unroll") for (int j = 0; j < 4; ++j)            \
          red2[((w * 4 + 2 * ps + q) * 4 + j) * 64 + lane] = acc[j][q];                                      \
      SB();                                                                                                  \
    }                                                                                                        \
  }
    if (mainw) {
      G2_BODY(NM, PF2M)
    } else {
      G2_BODY(NH, PF2)
    }
    STAMP(14);
    BAR();
    STAMP(27);
    f32x4 a[2];
    const f32x4* rd = (const f32x4*)smem + (qe * 4 + 2 * jp) * 64 + 32 * he + (lane & 31);
#pragma unroll
    for (int j = 0; j < 2; ++j) {
      f32x4 p[8];
#pragma unroll
      for (int ww = 0; ww < 8; ++ww) p[ww] = rd[(ww * 16 + j) * 64];
      a[j] = ((p[0] + p[1]) + (p[2] + p[3])) + ((p[4] + p[5]) + (p[6] + p[7]));
    }
    STAMP(28);
    u32 o[4];
    const f32x2 r2 = splat(__builtin_bit_cast(float, r2v));
#pragma unroll
    for (int r = 0; r < 4; ++r) {
      const u32 gw = *(const u32*)(smem + L_GB + (64 * ct2 + 16 * ge + 2 * jp + 4 * r) * 2);
      o[r] = pack2(fma2(bf_pair(gw), fma2(f32x2{a[0][r], a[1][r]}, r2, bf_pair(((const u32*)(smem + L_CS + 512))[8 * ge + jp + 2 * r])), bf_pair(x1v[r])));
    }
    const auto s02 = __builtin_amdgcn_permlane32_swap(o[0], o[2], false, false);
    const auto s13 = __builtin_amdgcn_permlane32_swap(o[1], o[3], false, false);
    *(GAS v4u*)(out + (size_t)te * D + 64 * ct2 + 16 * ge + 8 * jp) = v4u{(u32)s02[0], (u32)s02[1], (u32)s13[0], (u32)s13[1]};
  }
  asm volatile(".Lmega_end:");
}

extern "C" void kernel_launch(void* const* d_in, const int* in_sizes, int n_in, void* d_out, int out_size, void* d_ws, size_t ws_size,
                              hipStream_t stream) {
  (void)hipFuncSetAttribute((const void*)mega, hipFuncAttributeMaxDynamicSharedMemorySize, L_TOT);
  Params p;
  const int src[3][6] = {{1, 2, 0, 1, 3, 8}, {12, 12, 12, 12, 12, 11}, {0, 0, 4, 4, 4, 10}};
  for (int r = 0; r < 3; ++r)
    for (int i = 0; i < 6; ++i) {
      const int j = src[r][i];
      p.role[r][i].base = d_in[j];
      p.role[r][i].bytes = (u32)in_sizes[j] * 2u;
      p.role[r][i].flags = 0x00020000u;
    }
  for (int r = 0; r < 3; ++r) {
    p.role[r][6] = Desc{d_ws, (u32)WS_END, 0x00020000u};
    p.role[r][7] = Desc{d_in[7], (u32)(D * F * 2), 0x00020000u};
  }
  const void* rr[8] = {d_ws, d_ws, d_in[5], d_in[7], d_in[8], d_in[9], d_in[10], d_out};
  for (int i = 0; i < 8; ++i) p.rest[i] = rr[i];
  mega<<<256, 512, L_TOT, stream>>>(p);
}
```
